```python
import torch

def _softmax(x, axis=-1):
    return torch.softmax(x, dim=axis)
def _gelu(x):
    return torch.nn.functional.gelu(x, approximate="tanh")
def _silu(x):
    return torch.nn.functional.silu(x)
def _sigmoid(x):
    return torch.sigmoid(x)
def _layernorm(x, axis=-1, eps=1e-5):
    m = x.mean(dim=axis, keepdim=True)
    v = x.var(dim=axis, keepdim=True, unbiased=False)
    return (x - m) / torch.sqrt(v + eps)
def _scatter_add(tgt, idx, src, axis=0):
    return tgt.clone().index_add_(axis, idx, src)
def _segment_sum(src, seg, n):
    out = torch.zeros((n,) + src.shape[1:], dtype=src.dtype)
    return out.index_add_(0, seg, src)
def _pad(x, pw):
    flat = tuple(v for pair in reversed(pw) for v in pair)
    return torch.nn.functional.pad(x, flat)

kernel_name = 'sequential_preLN__sliding_block__dense_activation_relu__prompt'

def setup_inputs():

    _w = (lambda a: (lambda t: t.to(torch.bfloat16) if t.is_floating_point() else t)(torch.as_tensor(a)))
    def _A_setup():
        B, S, D = 1, 2048, 448
        return {
            "x": _w((lambda *s: torch.randn(*s, dtype=torch.bfloat16) * 0.05)(B, S, D)),
            "wqkv": _w((lambda *s: torch.randn(*s, dtype=torch.bfloat16) * 0.05)(D, 3 * D)),
            "wo": _w((lambda *s: torch.randn(*s, dtype=torch.bfloat16) * 0.05)(D, D)),
        }
    _A_in = _A_setup()

    def _B_setup():
        B, S, D, Dff = 1, 2048, 448, 1792
        return {
            "x": _w((lambda *s: torch.randn(*s, dtype=torch.bfloat16) * 0.05)(B, S, D)),
            "w1": _w((lambda *s: torch.randn(*s, dtype=torch.bfloat16) * 0.05)(D, Dff)),
            "w2": _w((lambda *s: torch.randn(*s, dtype=torch.bfloat16) * 0.05)(Dff, D)),
        }
    _B_in = _B_setup()

    return {
        "A_x": _A_in["x"],
        "A_wqkv": _A_in["wqkv"],
        "A_wo": _A_in["wo"],
        "B_x": _B_in["x"],
        "B_w1": _B_in["w1"],
        "B_w2": _B_in["w2"],
    }

def reference(A_x, A_wqkv, A_wo, B_x, B_w1, B_w2):
    def _A_ref(x, wqkv, wo):
        B, S, D = x.shape
        H = 16; Dh = D // H; W = 256
        qkv = (lambda x, *d: x.permute(*d))((x @ wqkv).reshape(B, S, 3, H, Dh), 2, 0, 3, 1, 4)
        q, k, v = qkv[0], qkv[1], qkv[2]
        scores = torch.einsum("bhqd,bhkd->bhqk", q, k) * (Dh ** -0.5)
        idx = torch.arange(S)
        mask = (idx[:, None] - idx[None, :] >= 0) & (idx[:, None] - idx[None, :] < W)
        scores = torch.where(mask[None, None], scores, -1e9)
        p = _softmax(scores, axis=-1)
        o = torch.einsum("bhqk,bhkd->bhqd", p, v)
        return (lambda x, *d: x.permute(*d))(o, 0, 2, 1, 3).reshape(B, S, D) @ wo

    def _B_ref(x, w1, w2):
        h = torch.einsum("bsd,df->bsf", x, w1)
        zero = torch.zeros_like(h)
        h = torch.maximum(h, zero)
        return torch.einsum("bsf,fd->bsd", h, w2)

    x = A_x
    h = x + _A_ref(_layernorm(x, axis=-1), A_wqkv, A_wo)
    h2 = h + _B_ref(_layernorm(h, axis=-1), B_w1, B_w2)
    return h2
```

```cpp
#include <hip/hip_runtime.h>
#include <hip/hip_bf16.h>
typedef __hip_bfloat16 bf16;
constexpr int S=2048, D=448, H=16, DH=28, W=256, DFF=1792;

__device__ inline float b2f(bf16 x){return __bfloat162float(x);}
__device__ inline bf16 f2b(float x){return __float2bfloat16(x);}

__global__ void ln_kernel(const bf16* x, bf16* y){
  int r=blockIdx.x; int t=threadIdx.x;
  float s=0; for(int i=t;i<D;i+=64) s+=b2f(x[r*D+i]);
  for(int o=32;o;o>>=1) s+=__shfl_xor(s,o);
  float m=s/D; float v=0;
  for(int i=t;i<D;i+=64){float d=b2f(x[r*D+i])-m; v+=d*d;}
  for(int o=32;o;o>>=1) v+=__shfl_xor(v,o);
  float rs=rsqrtf(v/D+1e-5f);
  for(int i=t;i<D;i+=64) y[r*D+i]=f2b((b2f(x[r*D+i])-m)*rs);
}
__global__ void gemm_kernel(const bf16* A,const bf16* B,const bf16* res,bf16* C,int N,int K,int relu){
  int n=blockIdx.x*64+threadIdx.x; int m=blockIdx.y;
  if(n>=N) return;
  float acc=0; for(int k=0;k<K;k++) acc+=b2f(A[m*K+k])*b2f(B[k*N+n]);
  if(relu) acc=fmaxf(acc,0.f);
  if(res) acc=b2f(f2b(acc))+b2f(res[m*N+n]);
  C[m*N+n]=f2b(acc);
}
__global__ void attn_kernel(const bf16* qkv, bf16* o){
  int q=blockIdx.x, h=blockIdx.y, t=threadIdx.x;
  __shared__ float p[256]; __shared__ float red[4];
  int k=q-t; float s=-1e30f;
  if(k>=0){ float a=0; for(int d=0;d<DH;d++) a+=b2f(qkv[q*3*D+h*DH+d])*b2f(qkv[k*3*D+D+h*DH+d]); s=a*rsqrtf((float)DH);}
  float m=s; for(int o2=32;o2;o2>>=1) m=fmaxf(m,__shfl_xor(m,o2));
  if((t&63)==0) red[t>>6]=m; __syncthreads();
  m=fmaxf(fmaxf(red[0],red[1]),fmaxf(red[2],red[3])); __syncthreads();
  float e=(k>=0)?__expf(s-m):0.f; float l=e; for(int o2=32;o2;o2>>=1) l+=__shfl_xor(l,o2);
  if((t&63)==0) red[t>>6]=l; __syncthreads();
  l=red[0]+red[1]+red[2]+red[3];
  p[t]=e/l; __syncthreads();
  if(t<DH){ float a=0; for(int j=0;j<256;j++){int kk=q-j; if(kk>=0) a+=p[j]*b2f(qkv[kk*3*D+2*D+h*DH+t]);} o[q*D+h*DH+t]=f2b(a);}
}
extern "C" void kernel_launch(void* const* d_in, const int* in_sizes, int n_in, void* d_out, int out_size, void* d_ws, size_t ws_size, hipStream_t stream){
  const bf16* x=(const bf16*)d_in[0]; const bf16* wqkv=(const bf16*)d_in[1]; const bf16* wo=(const bf16*)d_in[2];
  const bf16* w1=(const bf16*)d_in[4]; const bf16* w2=(const bf16*)d_in[5];
  bf16* ws=(bf16*)d_ws; bf16* xn=ws; bf16* qkv=xn+S*D; bf16* o=qkv+S*3*D; bf16* h=o+S*D; bf16* hn=h+S*D; bf16* f=hn+S*D;
  ln_kernel<<<S,64,0,stream>>>(x,xn);
  gemm_kernel<<<dim3(3*D/64,S),64,0,stream>>>(xn,wqkv,nullptr,qkv,3*D,D,0);
  attn_kernel<<<dim3(S,H),256,0,stream>>>(qkv,o);
  gemm_kernel<<<dim3(D/64,S),64,0,stream>>>(o,wo,x,h,D,D,0);
  ln_kernel<<<S,64,0,stream>>>(h,hn);
  gemm_kernel<<<dim3(DFF/64,S),64,0,stream>>>(hn,w1,nullptr,f,DFF,D,1);
  gemm_kernel<<<dim3(D/64,S),64,0,stream>>>(f,w2,h,(bf16*)d_out,D,DFF,0);
}
```

```cpp
#include <hip/hip_runtime.h>
#include <hip/hip_bf16.h>
#include <stdint.h>

#define GAS __attribute__((address_space(1)))
#define LAS __attribute__((address_space(3)))
typedef unsigned u32;
typedef unsigned long long u64;
typedef u32 u32x4 __attribute__((ext_vector_type(4)));
typedef u32 u32x2 __attribute__((ext_vector_type(2)));
typedef float f32x4 __attribute__((ext_vector_type(4)));
typedef float f32x2 __attribute__((ext_vector_type(2)));
typedef __bf16 bf16x8 __attribute__((ext_vector_type(8)));
typedef __bf16 bf16x2 __attribute__((ext_vector_type(2)));
typedef short s16x4 __attribute__((ext_vector_type(4)));
typedef short s16x2 __attribute__((ext_vector_type(2)));
typedef GAS u64 gu64;
#define RLX_AGENT __ATOMIC_RELAXED, __HIP_MEMORY_SCOPE_AGENT

namespace fb {
constexpr int S = 2048, D = 448, H = 16, DH = 28, DFF = 1792;
constexpr u32 FSTR = 64;
constexpr u32 WS_CTL = 0;
constexpr u32 WS_F1 = 4096;
constexpr u32 WS_F2 = WS_F1 + 2048 * FSTR;
constexpr u32 WS_F3 = WS_F2 + 2048 * FSTR;
constexpr u32 WS_F4 = WS_F3 + 1024 * FSTR;
constexpr u32 WS_KV = 1u << 20;
constexpr u32 WS_O = WS_KV + 16 * 128 * 2048;
constexpr u32 WS_HH = WS_O + 128 * 16 * 1024;
constexpr u32 WS_ST = WS_HH + 128 * 14 * 1024;
constexpr u32 WS_F = WS_ST + 128 * 8 * 128;
constexpr u32 WS_END = WS_F + 128 * 8 * 7168;
constexpr u32 WS_STAMP = 32u << 20;
constexpr u32 L_W = 0;
constexpr u32 L_XST = 86016;
constexpr u32 L_KV = 86016;
constexpr u32 L_WO = 0;
constexpr u32 L_W1 = 58368;
constexpr u32 L_MISC = 158720;
constexpr u32 L_CS = L_MISC;
constexpr u32 L_SX = L_MISC + 768;
constexpr u32 L_CS1 = L_MISC + 1792;
constexpr u32 L_PART = 0;
constexpr u32 L_HRES = 131072;
constexpr int SPIN_CAP = 1 << 16;

struct Params {
  const char* x; const char* wqkv; const char* wo; const char* w1; const char* w2; char* out; char* ws;
};

__device__ __forceinline__ f32x4 mfma(u32x4 a, u32x4 b, f32x4 c) {
  return __builtin_amdgcn_mfma_f32_16x16x32_bf16(__builtin_bit_cast(bf16x8, a), __builtin_bit_cast(bf16x8, b), c, 0, 0, 0);
}
__device__ __forceinline__ u32 pk(float lo, float hi) {
  bf16x2 v = {(__bf16)lo, (__bf16)hi};
  return __builtin_bit_cast(u32, v);
}
__device__ __forceinline__ float bflo(u32 v) { return __uint_as_float(v << 16); }
__device__ __forceinline__ float bfhi(u32 v) { return __uint_as_float(v & 0xffff0000u); }
__device__ __forceinline__ void glds16(const char* base, u32 voff, u32 lds) {
  u32 sv;
  asm volatile("s_mov_b32 %0, m0\n\ts_mov_b32 m0, %3\n\ts_nop 0\n\tglobal_load_lds_dwordx4 %1, %2\n\ts_mov_b32 m0, %0"
               : "=&s"(sv) : "v"(voff), "s"(base), "s"(lds) : "memory");
}
#define WAITV(n) asm volatile("s_waitcnt vmcnt(" #n ")" ::: "memory")
#define BAR() asm volatile("s_waitcnt lgkmcnt(0)\n\ts_barrier" ::: "memory")
__device__ __forceinline__ u32x2 trread(u32 addr) {
  s16x4 v = __builtin_amdgcn_ds_read_tr16_b64_v4i16((LAS s16x4*)addr);
  return __builtin_bit_cast(u32x2, v);
}
__device__ __forceinline__ u32x4 trfrag(u32 a0, u32 a1) {
  u32x2 lo = trread(a0), hi = trread(a1);
  return u32x4{lo.x, lo.y, hi.x, hi.y};
}
__device__ __forceinline__ u32x4 lds128(u32 a) { return *(LAS u32x4*)a; }
__device__ __forceinline__ u32x2 lds64(u32 a) { return *(LAS u32x2*)a; }
__device__ __forceinline__ float ldsf(u32 a) { return *(LAS float*)a; }
__device__ __forceinline__ f32x4 ldsf4(u32 a) { return *(LAS f32x4*)a; }
__device__ __forceinline__ void sts128(u32 a, u32x4 v) { *(LAS u32x4*)a = v; }
__device__ __forceinline__ void sts64(u32 a, u32x2 v) { *(LAS u32x2*)a = v; }
__device__ __forceinline__ void stsf(u32 a, float v) { *(LAS float*)a = v; }
__device__ __forceinline__ void stsf4(u32 a, f32x4 v) { *(LAS f32x4*)a = v; }
__device__ __forceinline__ float xor16(float v) { return __shfl_xor(v, 16); }
__device__ __forceinline__ float xor32(float v) { return __shfl_xor(v, 32); }

__device__ __forceinline__ void wait_flags(const char* ws, u32 off, u64 tag) {
  gu64* p = (gu64*)(ws + off);
  for (int it = 0; it < SPIN_CAP; ++it) {
    u64 v = __hip_atomic_load(p, RLX_AGENT);
    if (__all(v == tag)) break;
    __builtin_amdgcn_s_sleep(1);
  }
  asm volatile("" ::: "memory");
}
__device__ __forceinline__ void publish(char* ws, u32 off, u64 tag, int lane) {
  WAITV(0);
  if (lane == 0) __hip_atomic_store((gu64*)(ws + off), tag, RLX_AGENT);
}

#define STAMP(k) do {} while (0)

__global__ void __launch_bounds__(512, 2) fused_block(Params p) {
  __shared__ __attribute__((aligned(16))) char smem[163840];
  STAMP(0);
  const u32 L0 = (u32)(uintptr_t)(LAS char*)smem;
  const int tid = threadIdx.x, lane = tid & 63;
  const int w = __builtin_amdgcn_readfirstlane(tid >> 6);
  const int g4 = lane >> 4, i16 = lane & 15, q4 = (lane >> 2) & 3, p4 = lane & 3;
  const int b = blockIdx.x, grp = b & 7, idx = b >> 3;
  const int rb = 2 * grp + (idx >> 4), hc = idx & 15;
  const bool act3 = idx < 28;
  const int q3 = idx / 7, nt = idx - 7 * q3;
  const int tb = 8 * rb + w;
  char* ws = p.ws;
  const __amdgpu_buffer_rsrc_t rs = __builtin_amdgcn_make_buffer_rsrc(ws, 0, WS_END, 0x00020000);

  {
    const int tokl = lane >> 3, pos = lane & 7;
#pragma unroll
    for (int cc = 0; cc < 4; ++cc)
#pragma unroll
      for (int hf = 0; hf < 2; ++hf) {
        const int tok = 8 * hf + tokl;
        const u32 src = (u32)(16 * tb + tok) * 896u + cc * 128 + ((pos ^ ((tok >> 1) & 7)) * 16);
        glds16(p.x, src, L0 + L_XST + w * 8192 + (cc * 2 + hf) * 1024);
      }
  }
  const u32 wsh = (hc & 1) * 8;
  {
    const int part = w >> 2;
#pragma unroll
    for (int k = 0; k < 7; ++k) {
      const int row = (w & 3) * 112 + 16 * k + (lane >> 2);
      const int ch = (lane & 3) ^ (2 * ((row >> 3) & 1));
      const u32 src = (u32)row * 2688u + (part + 1) * 896 + hc * 56 - wsh + ch * 16;
      glds16(p.wqkv, src, L0 + L_W + (w * 7 + k) * 1024);
    }
  }
  u64 tag;
  {
    u64 e = __hip_atomic_load((gu64*)(ws + WS_CTL), RLX_AGENT);
    e += 0x9E3779B97F4A7C15ull;
    e = (e ^ (e >> 30)) * 0xBF58476D1CE4E5B9ull;
    e = (e ^ (e >> 27)) * 0x94D049BB133111EBull;
    e ^= e >> 31;
    u32 lo = __builtin_amdgcn_readfirstlane((u32)e), hi = __builtin_amdgcn_readfirstlane((u32)(e >> 32));
    tag = ((u64)hi << 32) | lo;
  }
  WAITV(0);
  STAMP(1);
  u32x4 xf[14];
  const u32 xa0 = L0 + L_XST + w * 8192 + (i16 >> 3) * 1024 + (i16 & 7) * 128;
  const u32 xo0 = xa0 + ((g4 ^ ((i16 >> 1) & 7)) * 16), xo1 = xa0 + (((4 + g4) ^ ((i16 >> 1) & 7)) * 16);
#pragma unroll
  for (int s = 0; s < 8; ++s) xf[s] = lds128(((s & 1) ? xo1 : xo0) + (s >> 1) * 2048);
  asm volatile("s_waitcnt lgkmcnt(0)" ::: "memory");
  {
    const int tokl = lane >> 3, pos = lane & 7;
#pragma unroll
    for (int cc = 4; cc < 7; ++cc)
#pragma unroll
      for (int hf = 0; hf < 2; ++hf) {
        const int tok = 8 * hf + tokl;
        const u32 src = (u32)(16 * tb + tok) * 896u + cc * 128 + ((pos ^ ((tok >> 1) & 7)) * 16);
        glds16(p.x, src, L0 + L_XST + w * 8192 + ((cc & 3) * 2 + hf) * 1024);
      }
#pragma unroll
    for (int k = 0; k < 4; ++k) {
      int pc = w + 8 * k; pc = pc > 27 ? 27 : pc;
      const int row = 16 * pc + (lane >> 2);
      const int ch = (lane & 3) ^ (2 * ((row >> 3) & 1));
      const u32 src = (u32)row * 2688u + hc * 56 - wsh + ch * 16;
      glds16(p.wqkv, src, L0 + L_W + 57344 + pc * 1024);
    }
  }
  {
    const u32 padb = (wsh + 56) & 63;
#pragma unroll
    for (int k = 0; k < 2; ++k) {
      const int r = 64 * k + lane;
      if (r < 112) {
        const int row = w * 112 + r;
        sts64(L0 + L_W + row * 64 + (padb ^ (32 * ((row >> 3) & 1))), u32x2{0x00003F80u, 0u});
      }
    }
  }
  BAR();
  STAMP(2);
  u32 offW[2];
#pragma unroll
  for (int t = 0; t < 2; ++t) offW[t] = L0 + L_W + (8 * g4 + q4) * 64 + (((wsh + 8 * (4 * t + p4)) & 63) ^ (32 * (g4 & 1)));
  const u32x4 ones = {0x3F803F80u, 0x3F803F80u, 0x3F803F80u, 0x3F803F80u};
  {
    const u32 a = ((w & 1) ? offW[1] : offW[0]) + ((w >> 1) & 1) * 28672 + (w >> 2) * 7 * 2048;
    f32x4 cs = {0, 0, 0, 0};
#pragma unroll
    for (int s = 0; s < 7; ++s) cs = mfma(trfrag(a + s * 2048, a + s * 2048 + 256), ones, cs);
    if (i16 == 0) stsf4(L0 + L_CS + ((w >> 2) * 6 + (w & 3)) * 64 + g4 * 16, cs);
  }
  f32x4 accK[2] = {{0, 0, 0, 0}, {0, 0, 0, 0}}, accV[2] = {{0, 0, 0, 0}, {0, 0, 0, 0}}, accG = {0, 0, 0, 0};
#pragma unroll
  for (int s = 0; s < 8; ++s) {
#pragma unroll
    for (int t = 0; t < 2; ++t) {
      accK[t] = mfma(trfrag(offW[t] + s * 2048, offW[t] + s * 2048 + 256), xf[s], accK[t]);
      accV[t] = mfma(xf[s], trfrag(offW[t] + 28672 + s * 2048, offW[t] + 28672 + s * 2048 + 256), accV[t]);
    }
    accG = mfma(xf[s], xf[s], accG);
  }
  WAITV(0);
#pragma unroll
  for (int s = 8; s < 14; ++s) xf[s] = lds128(((s & 1) ? xo1 : xo0) + ((s >> 1) & 3) * 2048);
#pragma unroll
  for (int s = 8; s < 14; ++s) {
#pragma unroll
    for (int t = 0; t < 2; ++t) {
      accK[t] = mfma(trfrag(offW[t] + s * 2048, offW[t] + s * 2048 + 256), xf[s], accK[t]);
      accV[t] = mfma(xf[s], trfrag(offW[t] + 28672 + s * 2048, offW[t] + 28672 + s * 2048 + 256), accV[t]);
    }
    accG = mfma(xf[s], xf[s], accG);
  }
  {
    const u32 padb = (wsh + 56) & 63;
    int pc = w + 8 * (lane >> 4); pc = pc > 27 ? 27 : pc;
    const int row = 16 * pc + i16;
    sts64(L0 + L_W + 57344 + row * 64 + (padb ^ (32 * ((row >> 3) & 1))), u32x2{0u, 0u});
  }
  {
    if (g4 == 3) stsf(L0 + L_SX + w * 128 + i16 * 4, accK[1][0]);
    float dgl = (i16 & 2) ? ((i16 & 1) ? accG[3] : accG[2]) : ((i16 & 1) ? accG[1] : accG[0]);
    if (g4 == (i16 >> 2)) stsf(L0 + L_SX + w * 128 + 64 + i16 * 4, dgl);
  }
  BAR();
  STAMP(3);
  float mean_i, rstd_i;
  {
    const float sx = ldsf(L0 + L_SX + w * 128 + i16 * 4), sq = ldsf(L0 + L_SX + w * 128 + 64 + i16 * 4);
    const f32x4 sx4 = ldsf4(L0 + L_SX + w * 128 + g4 * 16), sq4 = ldsf4(L0 + L_SX + w * 128 + 64 + g4 * 16);
    mean_i = sx * (1.0f / D);
    rstd_i = __builtin_amdgcn_rsqf(fmaxf(sq * (1.0f / D) - mean_i * mean_i, 0.f) + 1e-5f);
    u32x4 kf, vf;
#pragma unroll
    for (int t = 0; t < 2; ++t) {
      const f32x4 c = ldsf4(L0 + L_CS + t * 64 + g4 * 16) + ldsf4(L0 + L_CS + (6 + t) * 64 + g4 * 16);
      float kv[4];
#pragma unroll
      for (int r = 0; r < 4; ++r) kv[r] = rstd_i * (accK[t][r] - mean_i * c[r]);
      kf[2 * t] = pk(kv[0], kv[1]); kf[2 * t + 1] = pk(kv[2], kv[3]);
    }
    if (g4 == 3) { kf[2] = 0; kf[3] = 0; }
    float m4[4], r4[4];
#pragma unroll
    for (int r = 0; r < 4; ++r) {
      m4[r] = sx4[r] * (1.0f / D);
      r4[r] = __builtin_amdgcn_rsqf(fmaxf(sq4[r] * (1.0f / D) - m4[r] * m4[r], 0.f) + 1e-5f);
    }
#pragma unroll
    for (int t = 0; t < 2; ++t) {
      const float c = ldsf(L0 + L_CS + (2 + t) * 64 + i16 * 4) + ldsf(L0 + L_CS + (8 + t) * 64 + i16 * 4);
      float vv[4];
#pragma unroll
      for (int r = 0; r < 4; ++r) vv[r] = r4[r] * (accV[t][r] - m4[r] * c);
      vf[2 * t] = pk(vv[0], vv[1]); vf[2 * t + 1] = pk(vv[2], vv[3]);
    }
    if (i16 == 12) { vf[2] = 0x3F803F80u; vf[3] = 0x3F803F80u; }
    const u32 o = WS_KV + (u32)(hc * 128 + tb) * 2048u + lane * 16;
    __builtin_amdgcn_raw_buffer_store_b128(kf, rs, o, 0, 16);
    __builtin_amdgcn_raw_buffer_store_b128(vf, rs, o + 1024, 0, 16);
    const u32 sl = L0 + L_KV + (16 + w) * 2048;
    sts128(sl + lane * 16, kf);
    sts64(sl + 1024 + lane * 8, u32x2{vf[0], vf[1]});
    sts64(sl + 1536 + lane * 8, u32x2{vf[2], vf[3]});
    publish(ws, WS_F1 + (u32)(hc * 128 + tb) * FSTR, tag, lane);
  }
  STAMP(4);
  u32x4 qf;
  {
    if (w < 4) {
      const u32 a = ((w & 1) ? offW[1] : offW[0]) + 57344 + (w >> 1) * 7 * 2048;
      f32x4 cs = {0, 0, 0, 0};
#pragma unroll
      for (int s = 0; s < 7; ++s) cs = mfma(trfrag(a + s * 2048, a + s * 2048 + 256), ones, cs);
      if (i16 == 0) stsf4(L0 + L_CS + ((w >> 1) * 6 + 4 + (w & 1)) * 64 + g4 * 16, cs);
    }
    f32x4 accQ[2] = {{0, 0, 0, 0}, {0, 0, 0, 0}};
#pragma unroll
    for (int s = 0; s < 14; ++s)
#pragma unroll
      for (int t = 0; t < 2; ++t)
        accQ[t] = mfma(trfrag(offW[t] + 57344 + s * 2048, offW[t] + 57344 + s * 2048 + 256), xf[s], accQ[t]);
    const int hb = 8 * rb - 16 + 2 * w;
    u32x4 hv[4];
    if (hb >= 0) {
      wait_flags(ws, WS_F1 + (u32)(hc * 128 + hb + (lane & 1)) * FSTR, tag);
      const u32 o = WS_KV + (u32)(hc * 128 + hb) * 2048u + lane * 16;
#pragma unroll
      for (int k = 0; k < 4; ++k) hv[k] = __builtin_amdgcn_raw_buffer_load_b128(rs, o + k * 1024, 0, 16);
    } else {
      const u32 neg = (g4 == 3) ? 0x0000F149u : 0u;
      hv[0] = u32x4{0, 0, neg, 0}; hv[2] = hv[0];
      hv[1] = u32x4{0, 0, 0, 0}; hv[3] = hv[1];
    }
    const u32 sl = L0 + L_KV + 2 * w * 2048;
#pragma unroll
    for (int k = 0; k < 2; ++k) {
      sts128(sl + k * 2048 + lane * 16, hv[2 * k]);
      sts64(sl + k * 2048 + 1024 + lane * 8, u32x2{hv[2 * k + 1][0], hv[2 * k + 1][1]});
      sts64(sl + k * 2048 + 1536 + lane * 8, u32x2{hv[2 * k + 1][2], hv[2 * k + 1][3]});
    }
    BAR();
    STAMP(5);
    const float qs = rstd_i * (0.18898223650461362f * 1.4426950408889634f);
#pragma unroll
    for (int t = 0; t < 2; ++t) {
      const f32x4 c = ldsf4(L0 + L_CS + (4 + t) * 64 + g4 * 16) + ldsf4(L0 + L_CS + (10 + t) * 64 + g4 * 16);
      float qv[4];
#pragma unroll
      for (int r = 0; r < 4; ++r) qv[r] = qs * (accQ[t][r] - mean_i * c[r]);
      qf[2 * t] = pk(qv[0], qv[1]); qf[2 * t + 1] = pk(qv[2], qv[3]);
    }
    if (g4 == 3) { qf[2] = 0x00003F80u; qf[3] = 0; }
  }
  if (act3) {
#pragma unroll
    for (int k = 0; k < 8; ++k) {
      int pc = w + 8 * k; pc = pc > 56 ? 56 : pc;
      const int row = 8 * pc + (lane >> 3), pos = lane & 7;
      const int rowc = row > 447 ? 447 : row;
      const u32 src = (u32)rowc * 896u + nt * 128 + (((pos >> 1) ^ ((row >> 1) & 3)) * 32) + (pos & 1) * 16;
      glds16(p.wo, src, L0 + L_WO + pc * 1024);
    }
  }
#pragma unroll
  for (int k = 0; k < 7; ++k) {
    int e = w + 8 * k; e = e > 49 ? 49 : e;
    const int pc = e < 27 ? e : e + 48;
    const int ch = pc * 64 + lane, row = ch / 14, cc = ch - row * 14;
    glds16(p.w1, (u32)row * 3584u + hc * 224 + cc * 16, L0 + L_W1 + pc * 1024);
  }
  {
    f32x4 mlo, mhi;
#pragma unroll
    for (int r = 0; r < 4; ++r) {
      mlo[r] = (4 * g4 + r > i16) ? 0.f : -1e30f;
      mhi[r] = (4 * g4 + r <= i16) ? 0.f : -1e30f;
    }
    const u32 kb = L0 + L_KV + w * 2048 + lane * 16, vb = L0 + L_KV + w * 2048 + 1024 + lane * 8;
    f32x4 sc[17];
#pragma unroll
    for (int n = 0; n < 17; ++n) {
      const f32x4 c0 = n == 0 ? mlo : n == 16 ? mhi : f32x4{0, 0, 0, 0};
      sc[n] = mfma(lds128(kb + n * 2048), qf, c0);
    }
    float m = fmaxf(fmaxf(sc[0][0], sc[0][1]), fmaxf(sc[0][2], sc[0][3]));
#pragma unroll
    for (int n = 1; n < 17; ++n) {
      m = fmaxf(fmaxf(m, sc[n][0]), sc[n][1]);
      m = fmaxf(fmaxf(m, sc[n][2]), sc[n][3]);
    }
    m = fmaxf(m, xor16(m));
    m = fmaxf(m, xor32(m));
    u32x2 pp[18];
#pragma unroll
    for (int n = 0; n < 17; ++n) {
      float e[4];
#pragma unroll
      for (int r = 0; r < 4; ++r) e[r] = __builtin_amdgcn_exp2f(sc[n][r] - m);
      pp[n] = u32x2{pk(e[0], e[1]), pk(e[2], e[3])};
    }
    pp[17] = u32x2{0, 0};
    f32x4 oa[2] = {{0, 0, 0, 0}, {0, 0, 0, 0}};
#pragma unroll
    for (int n = 0; n < 18; n += 2) {
      const u32x4 pf = {pp[n].x, pp[n].y, pp[n + 1].x, pp[n + 1].y};
      const int n1 = n + 1 > 16 ? 16 : n + 1;
#pragma unroll
      for (int t = 0; t < 2; ++t) {
        const u32x2 va = lds64(vb + n * 2048 + t * 512), vc = lds64(vb + n1 * 2048 + t * 512);
        oa[t] = mfma(u32x4{va.x, va.y, vc.x, vc.y}, pf, oa[t]);
      }
    }
    const float l = __shfl(oa[1][0], 48 + i16);
    const float il = __builtin_amdgcn_rcpf(l);
    u32x4 of = {pk(oa[0][0] * il, oa[0][1] * il), pk(oa[0][2] * il, oa[0][3] * il),
                pk(oa[1][0] * il, oa[1][1] * il), pk(oa[1][2] * il, oa[1][3] * il)};
    if (g4 == 3) { of[2] = 0; of[3] = 0; }
    __builtin_amdgcn_raw_buffer_store_b128(of, rs, WS_O + (u32)(tb * 16 + hc) * 1024u + lane * 16, 0, 16);
    publish(ws, WS_F2 + (u32)(tb * 16 + hc) * FSTR, tag, lane);
  }
  STAMP(6);
  BAR();
#pragma unroll
  for (int k = 0; k < 6; ++k) {
    const int pc = 27 + w + 8 * k;
    const int ch = pc * 64 + lane, row = ch / 14, cc = ch - row * 14;
    glds16(p.w1, (u32)row * 3584u + hc * 224 + cc * 16, L0 + L_W1 + pc * 1024);
  }
  u32x2 hres[4] = {{0, 0}, {0, 0}, {0, 0}, {0, 0}};
  const int rb3 = 2 * grp + (q3 >> 1);
  if (act3 && w < 4) {
    const int tb3 = 8 * rb3 + 2 * (q3 & 1) + (w & 1) + 4 * (w >> 1);
    u32x2 xr[4];
#pragma unroll
    for (int t = 0; t < 4; ++t)
      xr[t] = *(const GAS u32x2*)(p.x + (size_t)(16 * tb3 + i16) * 896 + nt * 128 + t * 32 + g4 * 8);
    wait_flags(ws, WS_F2 + (u32)(tb3 * 16 + i16) * FSTR, tag);
    STAMP(7);
    u32x4 ofr[16];
#pragma unroll
    for (int k = 0; k < 16; ++k)
      ofr[k] = __builtin_amdgcn_raw_buffer_load_b128(rs, WS_O + (u32)(tb3 * 16 + k) * 1024u + lane * 16, 0, 16);
    u32 offO[4];
#pragma unroll
    for (int t = 0; t < 4; ++t) offO[t] = L0 + L_WO + (4 * g4 + q4) * 128 + ((t ^ ((2 * g4 + (q4 >> 1)) & 3)) * 32) + 8 * p4;
    f32x4 acc[4] = {{0, 0, 0, 0}, {0, 0, 0, 0}, {0, 0, 0, 0}, {0, 0, 0, 0}};
#pragma unroll
    for (int k = 0; k < 16; ++k)
#pragma unroll
      for (int t = 0; t < 4; ++t) {
        const u32 a = offO[t ^ (2 * (k & 1))] + k * 3584;
        acc[t] = mfma(trfrag(a, a + 2048), ofr[k], acc[t]);
      }
    float s1 = 0.f, s2 = 0.f;
#pragma unroll
    for (int t = 0; t < 4; ++t) {
      const u32 a01 = pk(acc[t][0], acc[t][1]), a23 = pk(acc[t][2], acc[t][3]);
      const u32 h01 = pk(bflo(xr[t].x) + bflo(a01), bfhi(xr[t].x) + bfhi(a01));
      const u32 h23 = pk(bflo(xr[t].y) + bflo(a23), bfhi(xr[t].y) + bfhi(a23));
      hres[t] = u32x2{h01, h23};
    }
#pragma unroll
    for (int u = 0; u < 2; ++u)
      __builtin_amdgcn_raw_buffer_store_b128(u32x4{hres[2 * u].x, hres[2 * u].y, hres[2 * u + 1].x, hres[2 * u + 1].y}, rs,
                                             WS_HH + (u32)(tb3 * 14 + 2 * nt + u) * 1024u + lane * 16, 0, 16);
#pragma unroll
    for (int t = 0; t < 4; ++t) {
      const float a = bflo(hres[t].x), bb = bfhi(hres[t].x), c = bflo(hres[t].y), d = bfhi(hres[t].y);
      s1 += (a + bb) + (c + d);
      s2 += (a * a + bb * bb) + (c * c + d * d);
    }
    s1 += xor16(s1); s2 += xor16(s2);
    s1 += xor32(s1); s2 += xor32(s2);
    if (g4 == 0)
      __builtin_amdgcn_raw_buffer_store_b64(u32x2{__float_as_uint(s1), __float_as_uint(s2)}, rs,
                                            WS_ST + (u32)(tb3 * 8 + nt) * 128u + i16 * 8, 0, 16);
    publish(ws, WS_F3 + (u32)(tb3 * 8 + nt) * FSTR, tag, lane);
    STAMP(8);
  }
  u32x4 w2f[7][4];
  if (act3) {
    const char* w2b = p.w2 + (size_t)(224 * w + 4 * g4) * 896 + nt * 128 + i16 * 8;
#pragma unroll
    for (int k = 0; k < 7; ++k) {
      u32x2 raw[8];
#pragma unroll
      for (int j = 0; j < 8; ++j) raw[j] = *(const GAS u32x2*)(w2b + (32 * k + 16 * (j >> 2) + (j & 3)) * 896);
#pragma unroll
      for (int t = 0; t < 4; ++t)
#pragma unroll
        for (int m = 0; m < 4; ++m)
          w2f[k][t][m] = __builtin_amdgcn_perm(raw[2 * m + 1][t >> 1], raw[2 * m][t >> 1], (t & 1) ? 0x07060302u : 0x05040100u);
    }
  } else {
#pragma unroll
    for (int k = 0; k < 7; ++k)
#pragma unroll
      for (int t = 0; t < 4; ++t) w2f[k][t] = u32x4{0, 0, 0, 0};
  }
  WAITV(0);
  BAR();
  STAMP(9);
  const u32 off1 = L0 + L_W1 + (4 * g4 + q4) * 224 + 8 * p4;
  if (w < 7) {
    f32x4 cs = {0, 0, 0, 0};
    const u32 a = off1 + w * 32;
#pragma unroll
    for (int s = 0; s < 14; ++s) cs = mfma(trfrag(a + s * 7168, a + s * 7168 + 3584), ones, cs);
    if (i16 == 0) stsf4(L0 + L_CS1 + w * 64 + g4 * 16, cs);
  }
  BAR();
  {
    wait_flags(ws, WS_F3 + (u32)(tb * 8 + (lane < 7 ? lane : 0)) * FSTR, tag);
    STAMP(10);
    u32x4 hf[14];
#pragma unroll
    for (int s = 0; s < 14; ++s)
      hf[s] = __builtin_amdgcn_raw_buffer_load_b128(rs, WS_HH + (u32)(tb * 14 + s) * 1024u + lane * 16, 0, 16);
    const u32x2 sa = __builtin_amdgcn_raw_buffer_load_b64(rs, WS_ST + (u32)(tb * 8 + g4) * 128u + i16 * 8, 0, 16);
    const u32x2 sb = __builtin_amdgcn_raw_buffer_load_b64(rs, WS_ST + (u32)(tb * 8 + (g4 < 3 ? g4 + 4 : 0)) * 128u + i16 * 8, 0, 16);
    f32x4 acc[7];
#pragma unroll
    for (int t = 0; t < 7; ++t) acc[t] = f32x4{0, 0, 0, 0};
#pragma unroll
    for (int s = 0; s < 14; ++s)
#pragma unroll
      for (int t = 0; t < 7; ++t) {
        const u32 a = off1 + s * 7168 + t * 32;
        acc[t] = mfma(trfrag(a, a + 3584), hf[s], acc[t]);
      }
    float s1 = __uint_as_float(sa.x) + (g4 < 3 ? __uint_as_float(sb.x) : 0.f);
    float s2 = __uint_as_float(sa.y) + (g4 < 3 ? __uint_as_float(sb.y) : 0.f);
    s1 += xor16(s1); s2 += xor16(s2);
    s1 += xor32(s1); s2 += xor32(s2);
    const float mean = s1 * (1.0f / D);
    const float rstd = __builtin_amdgcn_rsqf(fmaxf(s2 * (1.0f / D) - mean * mean, 0.f) + 1e-5f);
    u32x2 fo[7];
#pragma unroll
    for (int t = 0; t < 7; ++t) {
      const f32x4 c = ldsf4(L0 + L_CS1 + t * 64 + g4 * 16);
      float v[4];
#pragma unroll
      for (int r = 0; r < 4; ++r) v[r] = fmaxf(rstd * (acc[t][r] - mean * c[r]), 0.f);
      fo[t] = u32x2{pk(v[0], v[1]), pk(v[2], v[3])};
    }
    const u32 fb = WS_F + (u32)(tb * 8 + (hc >> 1)) * 7168u;
    if ((hc & 1) == 0) {
#pragma unroll
      for (int k = 0; k < 3; ++k)
        __builtin_amdgcn_raw_buffer_store_b128(u32x4{fo[2 * k].x, fo[2 * k].y, fo[2 * k + 1].x, fo[2 * k + 1].y}, rs, fb + k * 1024 + lane * 16, 0, 16);
      __builtin_amdgcn_raw_buffer_store_b64(fo[6], rs, fb + 3072 + lane * 8, 0, 16);
    } else {
      __builtin_amdgcn_raw_buffer_store_b64(fo[0], rs, fb + 3584 + lane * 8, 0, 16);
#pragma unroll
      for (int k = 0; k < 3; ++k)
        __builtin_amdgcn_raw_buffer_store_b128(u32x4{fo[2 * k + 1].x, fo[2 * k + 1].y, fo[2 * k + 2].x, fo[2 * k + 2].y}, rs, fb + 4096 + k * 1024 + lane * 16, 0, 16);
    }
    publish(ws, WS_F4 + (u32)(tb * 16 + hc) * FSTR, tag, lane);
    STAMP(11);
  }
  if (act3) {
    BAR();
    if (w < 4) {
#pragma unroll
      for (int t = 0; t < 4; ++t) sts64(L0 + L_HRES + w * 2048 + i16 * 128 + t * 32 + g4 * 8, hres[t]);
    }
    {
      const int mm = (lane >> 1) & 3;
      const int tbm = 8 * rb3 + 2 * (q3 & 1) + (mm & 1) + 4 * (mm >> 1);
      wait_flags(ws, WS_F4 + (u32)(tbm * 16 + 2 * w + (lane & 1)) * FSTR, tag);
    }
    STAMP(12);
#pragma unroll
    for (int m = 0; m < 4; ++m) {
      const int tbm = 8 * rb3 + 2 * (q3 & 1) + (m & 1) + 4 * (m >> 1);
      const u32 fb = WS_F + (u32)(tbm * 8 + w) * 7168u;
      u32x4 ff[7];
#pragma unroll
      for (int k = 0; k < 7; ++k) {
        if (k == 3) {
          const u32x2 a = __builtin_amdgcn_raw_buffer_load_b64(rs, fb + 3072 + lane * 8, 0, 16);
          const u32x2 c = __builtin_amdgcn_raw_buffer_load_b64(rs, fb + 3584 + lane * 8, 0, 16);
          ff[k] = u32x4{a.x, a.y, c.x, c.y};
        } else {
          ff[k] = __builtin_amdgcn_raw_buffer_load_b128(rs, fb + k * 1024 + lane * 16, 0, 16);
        }
      }
      f32x4 acc[4] = {{0, 0, 0, 0}, {0, 0, 0, 0}, {0, 0, 0, 0}, {0, 0, 0, 0}};
#pragma unroll
      for (int k = 0; k < 7; ++k)
#pragma unroll
        for (int t = 0; t < 4; ++t) acc[t] = mfma(w2f[k][t], ff[k], acc[t]);
#pragma unroll
      for (int t = 0; t < 4; ++t) {
        const u32 a = L0 + L_PART + ((w * 4 + m) * 4 + t) * 1024 + lane * 8;
        sts64(a, u32x2{__float_as_uint(acc[t][0]), __float_as_uint(acc[t][1])});
        sts64(a + 512, u32x2{__float_as_uint(acc[t][2]), __float_as_uint(acc[t][3])});
      }
    }
    STAMP(13);
    BAR();
    {
      const int m = w >> 1, rh = w & 1;
      float o[2][4];
#pragma unroll
      for (int t = 0; t < 4; ++t) {
        f32x2 s = {0, 0};
#pragma unroll
        for (int ww = 0; ww < 8; ++ww)
          s += *(LAS f32x2*)(L0 + L_PART + ((ww * 4 + m) * 4 + t) * 1024 + rh * 512 + lane * 8);
        o[0][t] = s.x; o[1][t] = s.y;
      }
      const u32x4 hr = lds128(L0 + L_HRES + m * 2048 + i16 * 128 + g4 * 32 + rh * 16);
      u32x4 res;
#pragma unroll
      for (int e = 0; e < 4; ++e) {
        const u32 y = pk(o[e >> 1][2 * (e & 1)], o[e >> 1][2 * (e & 1) + 1]);
        res[e] = pk(bflo(hr[e]) + bflo(y), bfhi(hr[e]) + bfhi(y));
      }
      const int tbm = 8 * rb3 + 2 * (q3 & 1) + (m & 1) + 4 * (m >> 1);
      *(GAS u32x4*)(p.out + (size_t)(16 * tbm + i16) * 896 + nt * 128 + g4 * 32 + rh * 16) = res;
    }
  } else if (b == 255 && w == 0) {
#pragma unroll
    for (int k = 0; k < 4; ++k) {
      const int wg = k * 64 + lane;
      wait_flags(ws, WS_F4 + (u32)((wg >> 4) * 8 * 16 + (wg & 15)) * FSTR, tag);
    }
    if (lane == 0) __hip_atomic_store((gu64*)(ws + WS_CTL), tag, RLX_AGENT);
  }
}
}

extern "C" void kernel_launch(void* const* d_in, const int* in_sizes, int n_in, void* d_out, int out_size, void* d_ws,
                              size_t ws_size, hipStream_t stream) {
  fb::Params p;
  p.x = (const char*)d_in[0]; p.wqkv = (const char*)d_in[1]; p.wo = (const char*)d_in[2];
  p.w1 = (const char*)d_in[4]; p.w2 = (const char*)d_in[5];
  p.out = (char*)d_out; p.ws = (char*)d_ws;
  fb::fused_block<<<256, 512, 0, stream>>>(p);
}
```

```cpp
#include <hip/hip_runtime.h>
#include <hip/hip_bf16.h>
#include <stdint.h>
#include <type_traits>

#define GAS __attribute__((address_space(1)))
#define LAS __attribute__((address_space(3)))
typedef unsigned u32;
typedef unsigned long long u64;
typedef u32 u32x4 __attribute__((ext_vector_type(4)));
typedef u32 u32x2 __attribute__((ext_vector_type(2)));
typedef float f32x4 __attribute__((ext_vector_type(4)));
typedef float f32x2 __attribute__((ext_vector_type(2)));
typedef __bf16 bf16x8 __attribute__((ext_vector_type(8)));
typedef __bf16 bf16x2 __attribute__((ext_vector_type(2)));
typedef short s16x4 __attribute__((ext_vector_type(4)));
typedef short s16x2 __attribute__((ext_vector_type(2)));
typedef GAS u64 gu64;
#define RLX_AGENT __ATOMIC_RELAXED, __HIP_MEMORY_SCOPE_AGENT

namespace fb {
constexpr int S = 2048, D = 448, H = 16, DH = 28, DFF = 1792;
constexpr u32 FSTR = 64;
constexpr u32 WS_CTL = 0;
constexpr u32 WS_F1 = 4096;
constexpr u32 WS_R1 = WS_F1 + 2048 * FSTR;
constexpr u32 WS_R2 = WS_R1 + 256 * FSTR;
constexpr u32 WS_G = WS_R2 + 256 * FSTR;
constexpr u32 WS_KV = 1u << 20;
constexpr u32 WS_O = WS_KV + 16 * 64 * 4096;
constexpr u32 WS_HH = WS_O + 128 * 16 * 1024;
constexpr u32 WS_ST = WS_HH + 128 * 14 * 1024;
constexpr u32 WS_F = WS_ST + 128 * 8 * 128;
constexpr u32 WS_END = WS_F + 128 * 8 * 7168;
constexpr u32 WS_OOB = 0x7FFFFF00u;
constexpr u32 WS_STAMP = 32u << 20;
constexpr u32 SENT = 0xFFFFFFFFu;
constexpr u32 SLOT = 24576;
constexpr u32 L_WQX = 98304;
constexpr u32 L_KV = 24576;
constexpr u32 L_Q = 73728;
constexpr u32 L_W1 = 0;
constexpr u32 L_WO = 102400;
constexpr u32 L_MISC = 159744;
constexpr u32 L_SX = L_MISC;
constexpr u32 L_CS1 = L_MISC;
constexpr u32 L_CSD = L_MISC + 1024;
constexpr u32 L_CSP = L_MISC + 2048;
constexpr u32 L_DUMMY = L_MISC + 3840;
constexpr u32 L_PART = 0;
constexpr u32 L_HRES = 131072;
constexpr int SPIN_CAP = 1 << 16;

struct Params {
  const char* x; const char* wqkv; const char* wo; const char* w1; const char* w2; char* out; char* ws;
};

__device__ __forceinline__ f32x4 mfma(u32x4 a, u32x4 b, f32x4 c) {
  return __builtin_amdgcn_mfma_f32_16x16x32_bf16(__builtin_bit_cast(bf16x8, a), __builtin_bit_cast(bf16x8, b), c, 0, 0, 0);
}
__device__ __forceinline__ u32 pk(float lo, float hi) {
  bf16x2 v = {(__bf16)lo, (__bf16)hi};
  return __builtin_bit_cast(u32, v);
}
__device__ __forceinline__ u32 san(u32 v) { return v < 0xFFFFFFFEu ? v : 0xFFFFFFFEu; }
__device__ __forceinline__ u32 relu2(u32 v) {
  s16x2 a = __builtin_bit_cast(s16x2, v), z = {0, 0};
  return __builtin_bit_cast(u32, __builtin_elementwise_max(a, z));
}
__device__ __forceinline__ float bflo(u32 v) { return __uint_as_float(v << 16); }
__device__ __forceinline__ float bfhi(u32 v) { return __uint_as_float(v & 0xffff0000u); }
__device__ __forceinline__ void glds16(const char* base, u32 voff, u32 lds) {
  u32 sv;
  asm volatile("s_mov_b32 %0, m0\n\ts_mov_b32 m0, %3\n\ts_nop 0\n\tglobal_load_lds_dwordx4 %1, %2\n\ts_mov_b32 m0, %0"
               : "=&s"(sv) : "v"(voff), "s"(base), "s"(lds) : "memory");
}
__device__ __forceinline__ void glds4(const char* base, u32 voff, u32 lds) {
  u32 sv;
  asm volatile("s_mov_b32 %0, m0\n\ts_mov_b32 m0, %3\n\ts_nop 0\n\tglobal_load_lds_dword %1, %2\n\ts_mov_b32 m0, %0"
               : "=&s"(sv) : "v"(voff), "s"(base), "s"(lds) : "memory");
}
template <int N> __device__ __forceinline__ void waitv() { asm volatile("s_waitcnt vmcnt(%0)" ::"n"(N) : "memory"); }
#define WAITV(n) asm volatile("s_waitcnt vmcnt(" #n ")" ::: "memory")
#define BAR() asm volatile("s_waitcnt lgkmcnt(0)\n\ts_barrier" ::: "memory")
#define SB() __builtin_amdgcn_sched_barrier(0)
__device__ __forceinline__ u32x2 trread(u32 addr) {
  s16x4 v = __builtin_amdgcn_ds_read_tr16_b64_v4i16((LAS s16x4*)addr);
  return __builtin_bit_cast(u32x2, v);
}
__device__ __forceinline__ u32x4 trfrag(u32 a0, u32 a1) {
  u32x2 lo = trread(a0), hi = trread(a1);
  return u32x4{lo.x, lo.y, hi.x, hi.y};
}
__device__ __forceinline__ u32x4 lds128(u32 a) { return *(LAS u32x4*)a; }
__device__ __forceinline__ f32x2 ldsf2(u32 a) { return *(LAS f32x2*)a; }
__device__ __forceinline__ f32x4 ldsf4(u32 a) { return *(LAS f32x4*)a; }
__device__ __forceinline__ void sts128(u32 a, u32x4 v) { *(LAS u32x4*)a = v; }
__device__ __forceinline__ void sts64(u32 a, u32x2 v) { *(LAS u32x2*)a = v; }
__device__ __forceinline__ void stsf2(u32 a, f32x2 v) { *(LAS f32x2*)a = v; }
__device__ __forceinline__ void stsf4(u32 a, f32x4 v) { *(LAS f32x4*)a = v; }
__device__ __forceinline__ float ldsf(u32 a) { return *(LAS float*)a; }
__device__ __forceinline__ float rows_sum(float v) {
  auto a = __builtin_amdgcn_permlane16_swap(__float_as_uint(v), __float_as_uint(v), false, false);
  v = __uint_as_float(a[0]) + __uint_as_float(a[1]);
  auto c = __builtin_amdgcn_permlane32_swap(__float_as_uint(v), __float_as_uint(v), false, false);
  return __uint_as_float(c[0]) + __uint_as_float(c[1]);
}
__device__ __forceinline__ float rows_max(float v) {
  auto a = __builtin_amdgcn_permlane16_swap(__float_as_uint(v), __float_as_uint(v), false, false);
  v = fmaxf(__uint_as_float(a[0]), __uint_as_float(a[1]));
  auto c = __builtin_amdgcn_permlane32_swap(__float_as_uint(v), __float_as_uint(v), false, false);
  return fmaxf(__uint_as_float(c[0]), __uint_as_float(c[1]));
}
template <int CTRL> __device__ __forceinline__ float dppf(float v) {
  return __uint_as_float(__builtin_amdgcn_update_dpp(0u, __float_as_uint(v), CTRL, 0xf, 0xf, false));
}
__device__ __forceinline__ float wave_sum(float v) {
  v += dppf<0x128>(v); v += dppf<0x124>(v); v += dppf<0x122>(v); v += dppf<0x121>(v);
  return rows_sum(v);
}
#define PIN(x) asm volatile("" : "+v"(x))
__device__ __forceinline__ u32x4 ald128(const char* base, u32 voff) {
  u32x4 v; asm volatile("global_load_dwordx4 %0, %1, %2" : "=v"(v) : "v"(voff), "s"(base) : "memory"); return v;
}
__device__ __forceinline__ u32x4 ald128s(const char* base, u32 voff) {
  u32x4 v; asm volatile("global_load_dwordx4 %0, %1, %2 sc1" : "=v"(v) : "v"(voff), "s"(base) : "memory"); return v;
}
__device__ __forceinline__ u32x2 ald64s(const char* base, u32 voff) {
  u32x2 v; asm volatile("global_load_dwordx2 %0, %1, %2 sc1" : "=v"(v) : "v"(voff), "s"(base) : "memory"); return v;
}
__device__ __forceinline__ float bperm(float v, int srclane) {
  return __uint_as_float(__builtin_amdgcn_ds_bpermute(srclane * 4, __float_as_uint(v)));
}
__device__ __forceinline__ u32 umax3(u32 a, u32 b, u32 c) { return max(max(a, b), c); }
__device__ __forceinline__ u32 chk(u32 bad, u32x4 v) { return umax3(umax3(bad, v.x, v.y), v.z, v.w); }

__device__ __forceinline__ void wait_flags(const char* ws, u32 off, u64 tag) {
  gu64* p = (gu64*)(ws + off);
#pragma nounroll
  for (int it = 0; it < SPIN_CAP; ++it) {
    u64 v = __hip_atomic_load(p, RLX_AGENT);
    if (__all(v == tag)) break;
  }
  asm volatile("" ::: "memory");
}
__device__ __forceinline__ void set_flag(char* ws, u32 off, u64 tag, int lane) {
  if (lane == 0) __hip_atomic_store((gu64*)(ws + off), tag, RLX_AGENT);
}
#define LD128(off) __builtin_amdgcn_raw_buffer_load_b128(rs, (off), 0, 16)
#define LD64(off) __builtin_amdgcn_raw_buffer_load_b64(rs, (off), 0, 16)
#define ST128(v, off) __builtin_amdgcn_raw_buffer_store_b128((v), rs, (off), 0, 16)
#define ST64(v, off) __builtin_amdgcn_raw_buffer_store_b64((v), rs, (off), 0, 16)
#define LDL128(soff) __builtin_amdgcn_raw_buffer_load_b128(rs, l16, (soff), 16)
#define LDL64(soff) __builtin_amdgcn_raw_buffer_load_b64(rs, l8, (soff), 16)
#define STL128(v, soff) __builtin_amdgcn_raw_buffer_store_b128((v), rs, l16, (soff), 16)
#define STL64(v, soff) __builtin_amdgcn_raw_buffer_store_b64((v), rs, l8, (soff), 16)
__device__ __forceinline__ void canary(const __amdgpu_buffer_rsrc_t rs, u32 off) {
#pragma nounroll
  for (int it = 0; it < SPIN_CAP; ++it) {
    asm volatile("" ::: "memory");
    const u32x4 v = LD128(off);
    if (__all(chk(0, v) != SENT)) break;
  }
  asm volatile("" ::: "memory");
}

template <int N, int L, class RD, class MM>
__device__ __forceinline__ void pipe(RD rd, MM mm) {
  u32x4 ring[L + 1];
#pragma unroll
  for (int j = 0; j < L; ++j) if (j < N) ring[j] = rd(j);
  SB();
#pragma unroll
  for (int j = 0; j < N; ++j) {
    if (j + L < N) ring[(j + L) % (L + 1)] = rd(j + L);
    mm(j, ring[j % (L + 1)]);
    SB();
  }
}
#define STAMP(k) do {} while (0)

__global__ void __launch_bounds__(512, 2) fused_block(Params p) {
  __shared__ __attribute__((aligned(16))) char smem[163840];
  STAMP(0);
  const u32 L0 = (u32)(uintptr_t)(LAS char*)smem;
  const int tid = threadIdx.x, lane = tid & 63;
  const int w = __builtin_amdgcn_readfirstlane(tid >> 6);
  const int g4 = lane >> 4, i16 = lane & 15, q4 = (lane >> 2) & 3, p4 = lane & 3;
  const int b = blockIdx.x, grp = b & 7, idx = b >> 3;
  const int rb = 2 * grp + (idx >> 4), hc = idx & 15;
  const bool act3 = hc < 14;
  const int q1 = hc >= 7 ? 1 : 0, nt = hc >= 14 ? hc - 14 : hc - 7 * q1;
  const int tb = 8 * rb + w;
  const int mp = w & 3, hsel = w >> 2, kind = hc & 1;
  const bool team = w >= 4;
  const bool teamact = team && act3;
  const u32 l16 = lane * 16, l8 = lane * 8;
  char* ws = p.ws;
  const __amdgpu_buffer_rsrc_t rs = __builtin_amdgcn_make_buffer_rsrc(ws, 0, WS_END, 0x00020000);
  const int rot = nt;
  const int tb30 = 8 * rb + 2 * q1;

  u32x2 ep = ald64s(ws, 0);
  u32x4 xrow = ald128(p.x + (size_t)(128 * rb + 8 * hc + w) * 896, (lane < 56 ? lane : 55) * 16);
  const u32 xsrc = (u32)(16 * tb + (lane >> 3)) * 896u;
  const u32 xs0 = xsrc + (((lane & 7) ^ (lane >> 4)) * 16), xs1 = xsrc + 8 * 896 + (((lane & 7) ^ (4 + (lane >> 4))) * 16);
  int wch = (lane & 7) ^ (2 * (((lane >> 4) & 1) | ((w & 1) << 1))); wch = wch > 6 ? 6 : wch;
  const u32 wsrc = (u32)(8 * w + (lane >> 3)) * 2688u + (kind + 1) * 896 + (hc & ~1) * 56 + wch * 16;
  auto issue_chunk = [&](int c) {
    int cc = c + rot; cc = cc >= 7 ? cc - 7 : cc;
    const u32 sl = L0 + ((c + 1) & 3) * SLOT;
    glds16(p.x + cc * 128, xs0, sl + w * 2048);
    glds16(p.x + cc * 128, xs1, sl + w * 2048 + 1024);
    glds16(p.wqkv + (size_t)(64 * cc * 2688), wsrc, sl + 16384 + w * 1024);
  };
  issue_chunk(0);
  __builtin_amdgcn_s_barrier();
  issue_chunk(1);
  issue_chunk(2);
  {
    u64 pc; u32 len;
    asm volatile("s_getpc_b64 %0\n.Lfb_code_begin:\n\ts_mov_b32 %1, .Lfb_code_end-.Lfb_code_begin" : "=s"(pc), "=s"(len));
    u32 off = (u32)(((idx & 7) * 8 + w) * 640 + (lane % 10) * 64);
    off = off < len ? off : 0;
    glds4((const char*)pc, off, L0 + L_DUMMY);
  }
  const u32 wsh = (hc & 1) * 8;
  u32 qsrc = (u32)(lane >> 2) * 2688u + hc * 56 - wsh + (((lane & 3) ^ (2 * ((lane >> 5) & 1))) * 16);
  const u32x4 ones = {0x3F803F80u, 0x3F803F80u, 0x3F803F80u, 0x3F803F80u};
  const u32x4 sent4 = {SENT, SENT, SENT, SENT};
  const u32 xa0 = L0 + mp * 4096 + (i16 >> 3) * 1024 + (i16 & 7) * 128;
  u32 xo[2] = {xa0 + ((g4 ^ ((i16 >> 1) & 7)) * 16), xa0 + (((4 + g4) ^ ((i16 >> 1) & 7)) * 16)};
  u32 offW[2];
#pragma unroll
  for (int t = 0; t < 2; ++t)
    offW[t] = L0 + 16384 + (8 * g4 + q4) * 128 + ((56 * hsel + 32 * t + 8 * p4) ^ (32 * (((q4 >> 1) & 1) | ((g4 & 1) << 1))));
  u32 qa = L0 + (8 * g4 + q4) * 64 + (((wsh + 8 * (4 * hsel + p4)) & 63) ^ (32 * (g4 & 1)));
  u32 osrc = (u32)(lane >> 3) * 896u + nt * 128 + ((((lane & 7) >> 1) ^ ((lane >> 4) & 3)) * 32) + (lane & 1) * 16;
  PIN(xo[0]); PIN(xo[1]); PIN(offW[0]); PIN(offW[1]); PIN(qa); PIN(osrc); PIN(qsrc);
  asm volatile("s_waitcnt vmcnt(10)" : "+v"(ep), "+v"(xrow) :: "memory");
  u64 tag;
  {
    u64 e = ((u64)(u32)__builtin_amdgcn_readfirstlane(ep.y) << 32) | (u32)__builtin_amdgcn_readfirstlane(ep.x);
    e += 0x9E3779B97F4A7C15ull;
    e = (e ^ (e >> 30)) * 0xBF58476D1CE4E5B9ull;
    e = (e ^ (e >> 27)) * 0x94D049BB133111EBull;
    tag = e ^ (e >> 31);
  }
  {
    float v[8];
#pragma unroll
    for (int d = 0; d < 4; ++d) { v[2 * d] = bflo(xrow[d]); v[2 * d + 1] = bfhi(xrow[d]); }
    float s = ((v[0] + v[1]) + (v[2] + v[3])) + ((v[4] + v[5]) + (v[6] + v[7]));
    s = lane < 56 ? s : 0.f;
    const float mean = wave_sum(s) * (1.0f / D);
    float q = 0.f;
#pragma unroll
    for (int d = 0; d < 8; ++d) { const float t = v[d] - mean; q += t * t; }
    q = lane < 56 ? q : 0.f;
    const float rstd = __builtin_amdgcn_rsqf(wave_sum(q) * (1.0f / D) + 1e-5f);
    ST128((u32x4{__float_as_uint(mean), (u32)tag, __float_as_uint(rstd), (u32)(tag >> 32)}),
          lane == 0 ? WS_G + (u32)(128 * rb + 8 * hc + w) * 16u : WS_OOB);
  }
  SB();
  u32x4 xf[7][2][2];
  u32x2 xr[4];
  auto p1_loop = [&](auto KIND) {
    constexpr int KD = decltype(KIND)::value;
    f32x4 acc1[2][2], csd[2], csr[2];
#pragma unroll
    for (int a = 0; a < 2; ++a) acc1[a][0] = acc1[a][1] = csd[a] = csr[a] = f32x4{0, 0, 0, 0};
    u32x4 bw[2][2][2];
    u32x4 gv;
    auto mm = [&](int c) {
#pragma unroll
      for (int u = 0; u < 2; ++u)
#pragma unroll
        for (int t = 0; t < 2; ++t) {
#pragma unroll
          for (int a = 0; a < 2; ++a)
            acc1[a][t] = KD == 0 ? mfma(bw[c & 1][u][t], xf[c][u][a], acc1[a][t]) : mfma(xf[c][u][a], bw[c & 1][u][t], acc1[a][t]);
          if (c == 6) csr[t] = KD == 0 ? mfma(bw[c & 1][u][t], ones, csr[t]) : mfma(ones, bw[c & 1][u][t], csr[t]);
        }
      if (c < 6 && mp == (c & 3)) {
#pragma unroll
        for (int u = 0; u < 2; ++u)
#pragma unroll
          for (int t = 0; t < 2; ++t) csd[t] = mfma(bw[c & 1][u][t], ones, csd[t]);
      }
      SB();
    };
#pragma unroll
    for (int c = 0; c < 7; ++c) {
      if (c < 3) waitv<8>(); else if (c < 5) waitv<6>(); else if (c == 5) waitv<4>();
      else {
        asm volatile("s_waitcnt vmcnt(0)" : "+v"(gv) :: "memory");
        if (__builtin_expect(!__all(gv.y == (u32)tag && gv.w == (u32)(tag >> 32)), 0)) {
#pragma nounroll
          for (int it = 0; it < SPIN_CAP; ++it) {
            gv = ald128s(ws + WS_G, (u32)(16 * tb + i16) * 16u);
            asm volatile("s_waitcnt vmcnt(0)" : "+v"(gv) :: "memory");
            if (__all(gv.y == (u32)tag && gv.w == (u32)(tag >> 32))) break;
          }
        }
        if (g4 == 0) stsf2(L0 + L_SX + (16 * w + i16) * 8, f32x2{__uint_as_float(gv.x), __uint_as_float(gv.z)});
      }
      BAR();
      if (c == 0) STAMP(1);
      if (c < 4 && !team) issue_chunk(c + 3);
      const u32 sl = ((c + 1) & 3) * SLOT;
#pragma unroll
      for (int u = 0; u < 2; ++u) {
#pragma unroll
        for (int a = 0; a < 2; ++a) xf[c][u][a] = lds128(xo[u] + sl + a * 2048);
#pragma unroll
        for (int t = 0; t < 2; ++t) {
          const u32 ad = offW[t] + sl + u * 4096;
          bw[c & 1][u][t] = trfrag(ad, ad + 512);
        }
      }
      if (c == 0) {
        const u32 xb = L0 + SLOT + (2 * q1 + (w & 1) + 4 * ((w >> 1) & 1)) * 2048 + (i16 >> 3) * 1024 + (i16 & 7) * 128 + (g4 & 1) * 8;
#pragma unroll
        for (int t = 0; t < 4; ++t) xr[t] = *(LAS u32x2*)(xb + (((2 * t + (g4 >> 1)) ^ ((i16 >> 1) & 7)) * 16));
      }
      SB();
      if (c == 4) gv = ald128s(ws + WS_G, (u32)(16 * tb + i16) * 16u);
      if (c >= 1 && c <= 4) mm(c - 1);
      if (c < 4 && team) issue_chunk(c + 3);
      if (c == 4 || c == 5) mm(c);
      if (c == 5) {
        if (i16 == 0) { stsf4(L0 + L_CSD + w * 128 + g4 * 16, csd[0]); stsf4(L0 + L_CSD + w * 128 + 64 + g4 * 16, csd[1]); }
      }
    }
    const u32 go = WS_KV + (u32)(((hc & ~1) + hsel) * 64 + 4 * rb + mp) * 4096u + KD * 2048;
    const u32 lo = L0 + L_KV + (8 + mp) * 4096 + KD * 2048 + lane * 16;
    if (KD == 0) {
      f32x4 cs[2]; f32x2 ms[2];
#pragma unroll
      for (int t = 0; t < 2; ++t) {
        const u32 a = L0 + L_CSD + hsel * 512 + t * 64 + g4 * 16;
        cs[t] = (ldsf4(a) + ldsf4(a + 128)) + (ldsf4(a + 256) + ldsf4(a + 384));
        ms[t] = ldsf2(L0 + L_SX + ((2 * mp + t) * 16 + i16) * 8);
      }
      mm(6);
#pragma unroll
      for (int a = 0; a < 2; ++a) {
        u32x4 kf;
#pragma unroll
        for (int t = 0; t < 2; ++t) {
          float kv[4];
#pragma unroll
          for (int r = 0; r < 4; ++r) kv[r] = ms[a].y * (acc1[a][t][r] - ms[a].x * (cs[t][r] + csr[t][r]));
          kf[2 * t] = pk(kv[0], kv[1]); kf[2 * t + 1] = pk(kv[2], kv[3]);
        }
        if (g4 == 3) { kf[2] = 0; kf[3] = 0; }
        STL128(kf, go + a * 1024);
        if (hsel == KD) sts128(lo + a * 1024, kf);
      }
    } else {
      float cs[2]; f32x4 ms[2][2];
#pragma unroll
      for (int t = 0; t < 2; ++t) {
        const u32 a = L0 + L_CSD + hsel * 512 + t * 64 + i16 * 4;
        cs[t] = (ldsf(a) + ldsf(a + 128)) + (ldsf(a + 256) + ldsf(a + 384));
        ms[t][0] = ldsf4(L0 + L_SX + ((2 * mp + t) * 16 + 4 * g4) * 8);
        ms[t][1] = ldsf4(L0 + L_SX + ((2 * mp + t) * 16 + 4 * g4) * 8 + 16);
      }
      mm(6);
      u32x4 vf[2];
#pragma unroll
      for (int a = 0; a < 2; ++a)
#pragma unroll
        for (int t = 0; t < 2; ++t) {
          float vv[4];
#pragma unroll
          for (int r = 0; r < 4; ++r)
            vv[r] = ms[a][r >> 1][2 * (r & 1) + 1] * (acc1[a][t][r] - ms[a][r >> 1][2 * (r & 1)] * (cs[t] + csr[t][0]));
          vf[t][2 * a] = pk(vv[0], vv[1]); vf[t][2 * a + 1] = pk(vv[2], vv[3]);
        }
      if (i16 >= 12) vf[1] = (i16 == 12) ? ones : u32x4{0, 0, 0, 0};
#pragma unroll
      for (int t = 0; t < 2; ++t) { STL128(vf[t], go + t * 1024); if (hsel == KD) sts128(lo + t * 1024, vf[t]); }
    }
  };
  if (kind == 0) p1_loop(std::integral_constant<int, 0>{}); else p1_loop(std::integral_constant<int, 1>{});
  SB();
  STAMP(2);
#pragma unroll
  for (int k = 0; k < 4; ++k) {
    int e = w + 8 * k; e = e > 27 ? 27 : e;
    int cc = (e >> 2) + rot; cc = cc >= 7 ? cc - 7 : cc;
    glds16(p.wqkv + (size_t)((64 * cc + 16 * (e & 3)) * 2688), qsrc, L0 + (e < 24 ? e * 1024 : L_WQX + (e - 24) * 1024));
  }
  WAITV(4);
  set_flag(ws, WS_F1 + (u32)((((hc & ~1) + hsel) * 64 + 4 * rb + mp) * 2 + kind) * FSTR, tag, lane);
  SB();
  {
    const u32 t3 = (u32)(tb30 + ((w >> 1) & 1) + 4 * (w >> 2));
    STL128(sent4, WS_O + (u32)(tb * 16 + hc) * 1024u);
    ST128(sent4, act3 ? WS_HH + (t3 * 14 + 2 * nt + (w & 1)) * 1024u + l16 : WS_OOB);
    ST64((u32x2{SENT, SENT}), (act3 && lane < 8) ? WS_ST + (t3 * 8 + nt) * 128u + (w & 1) * 64 + l8 : WS_OOB);
  }
  SB();
  WAITV(4);
  BAR();
  STAMP(3);
  {
    f32x4 aq[2] = {{0, 0, 0, 0}, {0, 0, 0, 0}}, cq = {0, 0, 0, 0};
    pipe<14, 3>([&](int j) { const u32 a = qa + (j < 12 ? j * 2048 : L_WQX + (j - 12) * 2048); return trfrag(a, a + 256); },
                [&](int j, u32x4 f) {
                  aq[0] = mfma(f, xf[j >> 1][j & 1][0], aq[0]);
                  aq[1] = mfma(f, xf[j >> 1][j & 1][1], aq[1]);
                  cq = mfma(f, ones, cq);
                  if (teamact) glds16(p.wo + (size_t)((mp + 4 * j) * 8 * 896), osrc, L0 + L_WO + (mp + 4 * j) * 1024);
                });
    STAMP(4);
    const u32 lq = L0 + L_Q + mp * 2048 + lane * 16 + hsel * 8;
#pragma unroll
    for (int a = 0; a < 2; ++a) {
      const f32x2 ms = ldsf2(L0 + L_SX + ((2 * mp + a) * 16 + i16) * 8);
      const float qs = ms.y * (0.18898223650461362f * 1.4426950408889634f);
      float qv[4];
#pragma unroll
      for (int r = 0; r < 4; ++r) qv[r] = qs * (aq[a][r] - ms.x * cq[r]);
      u32x2 qh = {pk(qv[0], qv[1]), pk(qv[2], qv[3])};
      if (hsel == 1 && g4 == 3) qh = u32x2{0x00003F80u, 0u};
      sts64(lq + a * 1024, qh);
    }
  }
  u32x4 hv[10];
  if (!team) {
    const int hp = 4 * rb - 8 + 2 * w;
    const u32 fpart = (u32)((hc * 64 + 4 * rb + w) * 2 + (kind ^ 1));
    const u32 opart = WS_KV + (u32)(hc * 64 + 4 * rb + w) * 4096u + (kind ^ 1) * 2048;
    if (hp >= 0) {
      wait_flags(ws, WS_F1 + ((lane & 4) ? fpart : (u32)((hc * 64 + hp) * 2 + (lane & 3))) * FSTR, tag);
      const u32 o = WS_KV + (u32)(hc * 64 + hp) * 4096u;
#pragma unroll
      for (int k = 0; k < 8; ++k) hv[k] = LDL128(o + k * 1024);
    } else {
      wait_flags(ws, WS_F1 + fpart * FSTR, tag);
      const u32 neg = (g4 == 3) ? 0x0000F149u : 0u;
#pragma unroll
      for (int k = 0; k < 8; ++k) hv[k] = (k & 2) ? u32x4{0, 0, 0, 0} : u32x4{0, 0, neg, 0};
    }
    hv[8] = LDL128(opart); hv[9] = LDL128(opart + 1024);
    SB();
  }
  f32x4 mk[4];
#pragma unroll
  for (int z = 0; z < 4; ++z) {
    const int n = z < 2 ? z : 14 + z;
#pragma unroll
    for (int r = 0; r < 4; ++r) {
      const int dist = 16 * (16 + (w & 1) - n) + i16 - 4 * g4 - r;
      mk[z][r] = ((u32)dist < 256u) ? 0.f : -1e30f;
    }
    PIN(mk[z]);
  }
  u32 w1src[5];
#pragma unroll
  for (int k = 0; k < 5; ++k) {
    int e = w + 8 * k; e = e > 34 ? 34 : e;
    const int pc = e < 21 ? e : e + 63;
    const int ch = pc * 64 + lane, row = ch / 14, cc = ch - row * 14;
    w1src[k] = (u32)row * 3584u + hc * 224 + cc * 16;
    PIN(w1src[k]);
  }
  const int kr = w ^ (4 * q1);
  const int e8 = (lane >> 3) & 1;
  u32 w2o = act3 ? (u32)(224 * kr + 4 * g4 + 32 * e8) * 896u + nt * 128 + (lane & 7) * 16 : WS_OOB;
  u32 w2o3 = act3 ? (u32)(224 * kr + 4 * g4 + 192 + 16 * e8) * 896u + nt * 128 + (lane & 7) * 16 : WS_OOB;
  PIN(w2o); PIN(w2o3);
  SB();
  if (!team) {
#pragma unroll
    for (int k = 0; k < 8; ++k) sts128(L0 + L_KV + w * 8192 + k * 1024 + lane * 16, hv[k]);
    sts128(L0 + L_KV + (8 + w) * 4096 + (kind ^ 1) * 2048 + lane * 16, hv[8]);
    sts128(L0 + L_KV + (8 + w) * 4096 + (kind ^ 1) * 2048 + 1024 + lane * 16, hv[9]);
  } else {
    if (act3) waitv<14>(); else waitv<0>();
  }
  BAR();
  STAMP(5);
  u32x4 raw[28];
  const __amdgpu_buffer_rsrc_t rw2 = __builtin_amdgcn_make_buffer_rsrc((void*)p.w2, 0, DFF * D * 2, 0x00020000);
  auto load_w2 = [&](int n) {
    if (n < 24) raw[n] = __builtin_amdgcn_raw_buffer_load_b128(rw2, w2o + (n & 3) * 896, (64 * (n >> 3) + 16 * ((n & 7) >> 2)) * 896, 0);
    else raw[n] = __builtin_amdgcn_raw_buffer_load_b128(rw2, w2o3 + (n - 24) * 896, 0, 0);
  };
  u64 r1v;
  {
    const u32x4 qf = lds128(L0 + L_Q + w * 1024 + lane * 16);
    const u32 fb = WS_F + (u32)(tb * 8 + (hc >> 1)) * 7168u;
    const u32 kb = L0 + L_KV + (w >> 1) * 4096 + lane * 16;
    f32x4 sc[18];
    float m0 = -3e38f, m1 = -3e38f;
    if (w == 7) set_flag(ws, WS_R1 + (u32)(rb * 16 + hc) * FSTR, tag, lane);
    pipe<18, 5>([&](int n) { return lds128(kb + (n >> 1) * 4096 + (n & 1) * 1024); },
                [&](int n, u32x4 f) {
                  const f32x4 c0 = n < 2 ? mk[n] : n >= 16 ? mk[n - 14] : f32x4{0, 0, 0, 0};
                  sc[n] = mfma(f, qf, c0);
                  if (n >= 2) { m0 = fmaxf(fmaxf(m0, sc[n - 2][0]), sc[n - 2][1]); m1 = fmaxf(fmaxf(m1, sc[n - 2][2]), sc[n - 2][3]); }
                  if (n == 1 || n == 5 || n == 9) STL128(sent4, fb + (hc & 1) * 4096 + (n >> 2) * 1024);
                  if (n == 13) STL64((u32x2{SENT, SENT}), fb + 3072 + (hc & 1) * 512);
                });
#pragma unroll
    for (int n = 16; n < 18; ++n) { m0 = fmaxf(fmaxf(m0, sc[n][0]), sc[n][1]); m1 = fmaxf(fmaxf(m1, sc[n][2]), sc[n][3]); }
    const float m = rows_max(fmaxf(m0, m1));
    f32x4 oa[2] = {{0, 0, 0, 0}, {0, 0, 0, 0}};
    u32x4 pf;
    auto bg = [&](int z) {
      if (z < 5) {
        int e = w + 8 * z; e = e > 34 ? 34 : e;
        glds16(p.w1, w1src[z], L0 + L_W1 + (e < 21 ? e : e + 63) * 1024);
      } else load_w2(z - 5);
    };
    pipe<18, 3>([&](int j) { return lds128(kb + (j >> 1) * 4096 + 2048 + (j & 1) * 1024); },
                [&](int j, u32x4 f) {
                  const int n = j & ~1, t = j & 1;
                  if (t == 0) {
                    bg(j);
                    pf[0] = pk(__builtin_amdgcn_exp2f(sc[n][0] - m), __builtin_amdgcn_exp2f(sc[n][1] - m));
                    pf[1] = pk(__builtin_amdgcn_exp2f(sc[n][2] - m), __builtin_amdgcn_exp2f(sc[n][3] - m));
                    SB();
                    bg(j + 1);
                    pf[2] = pk(__builtin_amdgcn_exp2f(sc[n + 1][0] - m), __builtin_amdgcn_exp2f(sc[n + 1][1] - m));
                    pf[3] = pk(__builtin_amdgcn_exp2f(sc[n + 1][2] - m), __builtin_amdgcn_exp2f(sc[n + 1][3] - m));
                  }
                  oa[t] = mfma(f, pf, oa[t]);
                });
    const float l = bperm(oa[1][0], 48 + i16);
    const float il = __builtin_amdgcn_rcpf(l);
    u32x4 of = {san(pk(oa[0][0] * il, oa[0][1] * il)), san(pk(oa[0][2] * il, oa[0][3] * il)),
                san(pk(oa[1][0] * il, oa[1][1] * il)), san(pk(oa[1][2] * il, oa[1][3] * il))};
    if (g4 == 3) { of[2] = 0; of[3] = 0; }
    STL128(of, WS_O + (u32)(tb * 16 + hc) * 1024u);
    SB();
  }
  STAMP(6);
  WAITV(14);
  BAR();
  u32x2 hres[4] = {{0, 0}, {0, 0}, {0, 0}, {0, 0}};
  const int tb3 = tb30 + (w & 1) + 4 * ((w >> 1) & 1);
  auto issue_w1 = [&](int pc) {
    const int ch = pc * 64 + lane, row = ch / 14, cc = ch - row * 14;
    glds16(p.w1, (u32)row * 3584u + hc * 224 + cc * 16, L0 + L_W1 + pc * 1024);
  };
  r1v = __hip_atomic_load((gu64*)(ws + WS_R1 + (u32)(rb * 16 + i16) * FSTR), RLX_AGENT);
  u32x4 w2f[7][4];
  auto conv_w2 = [&](int g) {
    u32x2 A[8], B[8];
#pragma unroll
    for (int j = 0; j < (g < 3 ? 8 : 4); ++j) {
      const u32x4 v = raw[8 * g + j];
#pragma unroll
      for (int d = 0; d < 2; ++d) {
        const u32 o1 = __builtin_amdgcn_update_dpp(v[d], v[2 + d], 0x128, 0xf, 0xc, false);
        const u32 o2 = __builtin_amdgcn_update_dpp(v[2 + d], v[d], 0x128, 0xf, 0x3, false);
        if (g < 3) { A[j][d] = o1; B[j][d] = o2; } else { A[j][d] = o1; A[4 + j][d] = o2; }
      }
    }
#pragma unroll
    for (int t = 0; t < 4; ++t) {
#pragma unroll
      for (int m = 0; m < 4; ++m) {
        w2f[2 * g][t][m] = __builtin_amdgcn_perm(A[2 * m + 1][t >> 1], A[2 * m][t >> 1], (t & 1) ? 0x07060302u : 0x05040100u);
        if (g < 3) w2f[2 * g + 1][t][m] = __builtin_amdgcn_perm(B[2 * m + 1][t >> 1], B[2 * m][t >> 1], (t & 1) ? 0x07060302u : 0x05040100u);
      }
      PIN(w2f[2 * g][t]);
      if (g < 3) PIN(w2f[2 * g + 1][t]);
    }
    SB();
  };
  if (team) {
    if (w == 7) set_flag(ws, WS_R2 + (u32)(rb * 16 + hc) * FSTR, tag, lane);
#pragma unroll
    for (int k = 0; k < 7; ++k) issue_w1((3 + mp) * 7 + k);
#pragma unroll
    for (int k = 0; k < 7; ++k) issue_w1((7 + mp) * 7 + k);
    if (mp == 3) {
#pragma unroll
      for (int k = 0; k < 7; ++k) issue_w1(77 + k);
    }
  }
  SB();
#pragma unroll
  for (int n = 13; n < 16; ++n) load_w2(n);
  SB();
  conv_w2(0);
  if (__builtin_expect(!__all(r1v == tag), 0)) wait_flags(ws, WS_R1 + (u32)(rb * 16 + i16) * FSTR, tag);
  if (team) {
    const u32 cvt_rd = L0 + L_W1 + (4 * g4 + q4) * 224 + 8 * p4;
    f32x4 c1[7];
#pragma unroll
    for (int t = 0; t < 7; ++t) c1[t] = f32x4{0, 0, 0, 0};
#pragma nounroll
    for (int k = 0; k < 5; ++k) {
      int s;
      if (k == 0) { if (mp == 3) continue; s = mp; }
      else if (k == 1) { if (mp >= 2) continue; s = 12 + mp; }
      else if (k == 2) { WAITV(0); s = 3 + mp; }
      else if (k == 3) s = 7 + mp;
      else { if (mp != 3) continue; s = 11; }
      u32x4 fr[7];
      const u32 a = cvt_rd + s * 7168;
#pragma unroll
      for (int t = 0; t < 7; ++t) {
        const u32 at = a + (t < 6 ? t * 32 + (hc & 1) * 32 : ((hc & 1) ? 0 : 192));
        fr[t] = trfrag(at, at + 3584);
      }
      asm volatile("s_waitcnt lgkmcnt(0)" ::: "memory");
      SB();
#pragma unroll
      for (int t = 0; t < 7; ++t) {
        sts128(L0 + L_W1 + s * 7168 + t * 1024 + lane * 16, fr[t]);
        c1[t] = mfma(fr[t], ones, c1[t]);
      }
      SB();
    }
    if (i16 == 0) {
#pragma unroll
      for (int t = 0; t < 7; ++t) stsf4(L0 + L_CSP + (mp * 7 + t) * 64 + g4 * 16, c1[t]);
    }
  } else if (act3) {
    u32 offO[4], offL[4];
#pragma unroll
    for (int t = 0; t < 4; ++t) {
      offO[t] = L0 + L_WO + (4 * g4 + q4) * 128 + ((t ^ ((2 * g4 + (q4 >> 1)) & 3)) * 32) + 8 * p4;
      offL[t] = offO[t] - (g4 == 3 ? 2048 : 0);
      PIN(offO[t]); PIN(offL[t]);
    }
    canary(rs, WS_O + (u32)(tb3 * 16 + (lane >> 2)) * 1024u + 960 + (lane & 3) * 16);
    STAMP(7);
    f32x4 acc[4];
#pragma nounroll
    for (int it = 0; it < SPIN_CAP; ++it) {
      asm volatile("" ::: "memory");
      constexpr int PD = 8;
      u32x4 ofr[16];
#pragma unroll
      for (int k = 0; k < PD; ++k) ofr[k] = LDL128(WS_O + (u32)(tb3 * 16 + k) * 1024u);
      SB();
      u32 bad = 0;
#pragma unroll
      for (int t = 0; t < 4; ++t) acc[t] = f32x4{0, 0, 0, 0};
      pipe<64, 5>(
          [&](int j) {
            const int k = j >> 2, t = j & 3;
            const u32 a = offO[t ^ (2 * (k & 1))] + k * 3584;
            return trfrag(a, (k == 15 ? offL[t ^ 2] + k * 3584 : a) + 2048);
          },
          [&](int j, u32x4 f) {
            acc[j & 3] = mfma(f, ofr[j >> 2], acc[j & 3]);
            if ((j & 3) == 0 && (j >> 2) + PD < 16) ofr[(j >> 2) + PD] = LDL128(WS_O + (u32)(tb3 * 16 + (j >> 2) + PD) * 1024u);
            if ((j & 3) == 1) bad = chk(bad, ofr[j >> 2]);
          });
      if (__all(bad != SENT)) break;
    }
#pragma unroll
    for (int t = 0; t < 4; ++t) {
      const u32 a01 = pk(acc[t][0], acc[t][1]), a23 = pk(acc[t][2], acc[t][3]);
      const u32 h01 = pk(bflo(xr[t].x) + bflo(a01), bfhi(xr[t].x) + bfhi(a01));
      const u32 h23 = pk(bflo(xr[t].y) + bflo(a23), bfhi(xr[t].y) + bfhi(a23));
      hres[t] = u32x2{san(h01), san(h23)};
    }
#pragma unroll
    for (int u = 0; u < 2; ++u)
      STL128((u32x4{hres[2 * u].x, hres[2 * u].y, hres[2 * u + 1].x, hres[2 * u + 1].y}),
             WS_HH + (u32)(tb3 * 14 + 2 * nt + u) * 1024u);
    SB();
    float s1 = 0.f, s2 = 0.f;
#pragma unroll
    for (int t = 0; t < 4; ++t) {
      const float a = bflo(hres[t].x), bb = bfhi(hres[t].x), c = bflo(hres[t].y), d = bfhi(hres[t].y);
      s1 += (a + bb) + (c + d);
      s2 += (a * a + bb * bb) + (c * c + d * d);
    }
    s1 = rows_sum(s1); s2 = rows_sum(s2);
    if (g4 == 0)
      ST64((u32x2{san(__float_as_uint(s1)), san(__float_as_uint(s2))}), WS_ST + (u32)(tb3 * 8 + nt) * 128u + i16 * 8);
    SB();
    STAMP(8);
  }
#pragma unroll
  for (int n = 16; n < 28; ++n) load_w2(n);
  SB();
  conv_w2(1);
  BAR();
  STAMP(9);
  u64 r2v;
  {
    {
      const int pcs = lane < 28 ? lane : 27;
      const u32 a = L0 + L_CSP + pcs * 16;
      stsf4(L0 + L_CS1 + pcs * 16, (ldsf4(a) + ldsf4(a + 448)) + (ldsf4(a + 896) + ldsf4(a + 1344)));
    }
    const int cl = lane < 56 ? lane : lane - 56;
    canary(rs, WS_HH + (u32)(tb * 14 + (cl >> 2)) * 1024u + 960 + (cl & 3) * 16);
    STAMP(10);
    u32x4 hf[14];
    f32x4 acc[7];
    const u32 w1a = L0 + L_W1 + lane * 16;
    const u32 sto0 = WS_ST + (u32)(tb * 8 + g4) * 128u + i16 * 8, sto1 = WS_ST + (u32)(tb * 8 + (g4 < 3 ? g4 + 4 : 0)) * 128u + i16 * 8;
    u32x2 sa, sb;
#pragma nounroll
    for (int it = 0; it < SPIN_CAP; ++it) {
      asm volatile("" ::: "memory");
      constexpr int PD = 6;
#pragma unroll
      for (int s = 0; s < PD; ++s) hf[s] = LDL128(WS_HH + (u32)(tb * 14 + s) * 1024u);
      SB();
      u32 bad = 0;
#pragma unroll
      for (int t = 0; t < 4; ++t) acc[t] = f32x4{0, 0, 0, 0};
      pipe<56, 4>([&](int j) { return lds128(w1a + (j >> 2) * 7168 + (j & 3) * 1024); },
                  [&](int j, u32x4 f) {
                    acc[j & 3] = mfma(f, hf[j >> 2], acc[j & 3]);
                    if ((j & 3) == 0 && (j >> 2) + PD < 14) hf[(j >> 2) + PD] = LDL128(WS_HH + (u32)(tb * 14 + (j >> 2) + PD) * 1024u);
                    if (j == 36) sa = LD64(sto0);
                    if (j == 40) sb = LD64(sto1);
                    if ((j & 3) == 1) bad = chk(bad, hf[j >> 2]);
                  });
      bad = umax3(umax3(bad, sa.x, sa.y), sb.x, sb.y);
      if (__all(bad != SENT)) break;
    }
    r2v = __hip_atomic_load((gu64*)(ws + WS_R2 + (u32)(rb * 16 + i16) * FSTR), RLX_AGENT);
    const float s1 = rows_sum(__uint_as_float(sa.x) + (g4 < 3 ? __uint_as_float(sb.x) : 0.f));
    const float s2 = rows_sum(__uint_as_float(sa.y) + (g4 < 3 ? __uint_as_float(sb.y) : 0.f));
    const float mean = s1 * (1.0f / D);
    const float rstd = __builtin_amdgcn_rsqf(fmaxf(s2 * (1.0f / D) - mean * mean, 0.f) + 1e-5f);
    const float nmr = -mean * rstd;
    const u32 fb = WS_F + (u32)(tb * 8 + (hc >> 1)) * 7168u;
    auto epi = [&](int t) {
      const f32x4 c1 = ldsf4(L0 + L_CS1 + t * 64 + g4 * 16);
      return u32x2{relu2(pk(rstd * acc[t][0] + nmr * c1[0], rstd * acc[t][1] + nmr * c1[1])),
                   relu2(pk(rstd * acc[t][2] + nmr * c1[2], rstd * acc[t][3] + nmr * c1[3]))};
    };
#pragma unroll
    for (int t = 4; t < 7; ++t) acc[t] = f32x4{0, 0, 0, 0};
    pipe<42, 4>([&](int j) { const int s = j / 3, t = j - 3 * s; return lds128(w1a + s * 7168 + (4 + t) * 1024); },
                [&](int j, u32x4 f) {
                  const int s = j / 3, t = j - 3 * s;
                  acc[4 + t] = mfma(f, hf[s], acc[4 + t]);
                  if (j == 2 || j == 10) {
                    const int k = j == 2 ? 0 : 1;
                    const u32x2 a = epi(2 * k), c = epi(2 * k + 1);
                    STL128((u32x4{a.x, a.y, c.x, c.y}), fb + (hc & 1) * 4096 + k * 1024);
                  }
                });
    {
      const u32x2 a = epi(4), c = epi(5), d = epi(6);
      STL128((u32x4{a.x, a.y, c.x, c.y}), fb + (hc & 1) * 4096 + 2048);
      STL64(d, fb + 3072 + (hc & 1) * 512);
    }
    SB();
    STAMP(11);
  }
  if (act3) {
    conv_w2(2);
    conv_w2(3);
    if (__builtin_expect(!__all(r2v == tag), 0)) wait_flags(ws, WS_R2 + (u32)(rb * 16 + i16) * FSTR, tag);
    BAR();
    if (w < 4) {
#pragma unroll
      for (int t = 0; t < 4; ++t) sts64(L0 + L_HRES + w * 2048 + i16 * 128 + t * 32 + g4 * 8, hres[t]);
    }
    {
      const int mm = lane >> 4, pcs = (lane >> 1) & 7;
      const u32 endb = pcs < 3 ? (pcs + 1) * 1024 : pcs == 3 ? 3584 : pcs * 1024;
      canary(rs, WS_F + (u32)((tb30 + (mm & 1) + 4 * (mm >> 1)) * 8 + kr) * 7168u + endb - 32 + (lane & 1) * 16);
    }
    STAMP(12);
    const u32 pw = L0 + L_PART + w * 16384 + lane * 8;
#pragma nounroll
    for (int it = 0; it < SPIN_CAP; ++it) {
      asm volatile("" ::: "memory");
      constexpr int PD = 10, RG = PD + 1;
      u32x4 ff[RG];
      auto ldf = [&](int j) {
        const int m = j / 7, k = j - 7 * m;
        const u32 fb = WS_F + (u32)((tb30 + (m & 1) + 4 * (m >> 1)) * 8 + kr) * 7168u;
        if (k == 3) {
          const u32x2 a = LDL64(fb + 3072), c = LDL64(fb + 3584);
          ff[j % RG] = u32x4{a.x, a.y, c.x, c.y};
        } else ff[j % RG] = LDL128(fb + k * 1024);
      };
#pragma unroll
      for (int j = 0; j < PD; ++j) ldf(j);
      SB();
      f32x4 acc[2][4];
      auto wr = [&](int m, int t) {
        stsf2(pw + (m * 4 + t) * 1024, f32x2{acc[m & 1][t][0], acc[m & 1][t][1]});
        stsf2(pw + (m * 4 + t) * 1024 + 512, f32x2{acc[m & 1][t][2], acc[m & 1][t][3]});
      };
      u32 bad = 0;
#pragma unroll
      for (int j = 0; j < 28; ++j) {
        const int m = j / 7, k = j - 7 * m;
        if (j + PD < 28) ldf(j + PD);
#pragma unroll
        for (int t = 0; t < 4; ++t) acc[m & 1][t] = mfma(w2f[k][t], ff[j % RG], k == 0 ? f32x4{0, 0, 0, 0} : acc[m & 1][t]);
        bad = chk(bad, ff[j % RG]);
        if (m > 0 && k >= 1 && k <= 4) wr(m - 1, k - 1);
        SB();
      }
#pragma unroll
      for (int t = 0; t < 4; ++t) wr(3, t);
      if (__all(bad != SENT)) break;
    }
    STAMP(13);
    BAR();
    {
      const int m = w >> 1, rh = w & 1;
      float o[2][4];
#pragma unroll
      for (int t = 0; t < 4; ++t) {
        f32x2 s = {0, 0};
#pragma unroll
        for (int ww = 0; ww < 8; ++ww) s += ldsf2(L0 + L_PART + ((ww * 4 + m) * 4 + t) * 1024 + rh * 512 + lane * 8);
        o[0][t] = s.x; o[1][t] = s.y;
      }
      u32 y0[2] = {pk(o[0][0], o[0][1]), pk(o[0][2], o[0][3])}, y1[2] = {pk(o[1][0], o[1][1]), pk(o[1][2], o[1][3])};
      u32x4 y;
#pragma unroll
      for (int d = 0; d < 2; ++d) {
        auto sw = __builtin_amdgcn_permlane32_swap(y0[d], y1[d], false, false);
        y[d] = sw[0]; y[2 + d] = sw[1];
      }
      const int colb = (32 * (g4 & 1) + 8 * (2 * rh + (g4 >> 1))) * 2;
      const u32x4 hr = lds128(L0 + L_HRES + m * 2048 + i16 * 128 + colb);
      u32x4 res;
#pragma unroll
      for (int e = 0; e < 4; ++e) res[e] = pk(bflo(hr[e]) + bflo(y[e]), bfhi(hr[e]) + bfhi(y[e]));
      *(GAS u32x4*)(p.out + (size_t)(16 * (tb30 + (m & 1) + 4 * (m >> 1)) + i16) * 896 + nt * 128 + colb) = res;
    }
  } else if (b == 255 && w == 0) {
#pragma nounroll
    for (int k = 0; k < 4; ++k) wait_flags(ws, WS_R2 + (u32)(k * 64 + lane) * FSTR, tag);
    if (lane == 0) __hip_atomic_store((gu64*)(ws + WS_CTL), tag, RLX_AGENT);
  }
  asm volatile(".Lfb_code_end:" ::: "memory");
}
}

extern "C" void kernel_launch(void* const* d_in, const int* in_sizes, int n_in, void* d_out, int out_size, void* d_ws,
                              size_t ws_size, hipStream_t stream) {
  fb::Params p;
  p.x = (const char*)d_in[0]; p.wqkv = (const char*)d_in[1]; p.wo = (const char*)d_in[2];
  p.w1 = (const char*)d_in[4]; p.w2 = (const char*)d_in[5];
  p.out = (char*)d_out; p.ws = (char*)d_ws;
  fb::fused_block<<<256, 512, 0, stream>>>(p);
}
```

```cpp
#include <hip/hip_runtime.h>
#include <hip/hip_bf16.h>
#include <stdint.h>
#include <type_traits>

#define GAS __attribute__((address_space(1)))
#define LAS __attribute__((address_space(3)))
typedef unsigned u32;
typedef unsigned long long u64;
typedef u32 u32x4 __attribute__((ext_vector_type(4)));
typedef u32 u32x2 __attribute__((ext_vector_type(2)));
typedef float f32x4 __attribute__((ext_vector_type(4)));
typedef float f32x2 __attribute__((ext_vector_type(2)));
typedef __bf16 bf16x8 __attribute__((ext_vector_type(8)));
typedef __bf16 bf16x2 __attribute__((ext_vector_type(2)));
typedef short s16x4 __attribute__((ext_vector_type(4)));
typedef short s16x2 __attribute__((ext_vector_type(2)));
typedef GAS u64 gu64;
#define RLX_AGENT __ATOMIC_RELAXED, __HIP_MEMORY_SCOPE_AGENT

namespace fb {
constexpr int S = 2048, D = 448, H = 16, DH = 28, DFF = 1792;
constexpr u32 FSTR = 64;
constexpr u32 RBS = 1u << 20;
constexpr u32 WS_CTL = 0;
constexpr u32 B_F1 = 0x400;
constexpr u32 B_R1 = 0x1400;
constexpr u32 B_G = 0x1800;
constexpr u32 B_F4 = 0x2000;
constexpr u32 B_ST = 0x4000;
constexpr u32 B_KV = 0x10000;
constexpr u32 B_O = 0x50000;
constexpr u32 B_HH = 0x70000;
constexpr u32 B_F = 0x90000;
constexpr u32 WS_END = 16 * RBS;
constexpr u32 WS_OOB = 0x7FFFFF00u;
constexpr u32 WS_STAMP = 32u << 20;
constexpr u32 SENT = 0xFFFFFFFFu;
constexpr u32 SLOT = 16384;
constexpr u32 L_WKV = 102400;
constexpr u32 L_WQ = 16384;
constexpr u32 L_KV = 53248;
constexpr u32 L_Q = 0;
constexpr u32 L_W1 = 0;
constexpr u32 L_WO = 102400;
constexpr u32 L_MISC = 159744;
constexpr u32 L_SX = L_MISC;
constexpr u32 L_CS1 = L_MISC;
constexpr u32 L_CS = L_MISC + 1024;
constexpr u32 L_CQ = L_MISC + 1280;
constexpr u32 L_CNT = L_MISC + 1536;
constexpr u32 L_TAG = L_MISC + 1664;
constexpr u32 L_CSP = L_MISC + 2048;
constexpr u32 L_DUMMY = L_MISC + 3840;
constexpr u32 L_PART = 0;
constexpr u32 L_HRES = 131072;
enum { C_CH = 0, C_RD = 8, C_CS = 16, C_WQ, C_CQ, C_GO, C_KV };
constexpr int SPIN_CAP = 1 << 16;
#define WARM_J 4

struct Params {
  const char* x; const char* wqkv; const char* wo; const char* w1; const char* w2; char* out; char* ws;
};

__device__ __forceinline__ f32x4 mfma(u32x4 a, u32x4 b, f32x4 c) {
  return __builtin_amdgcn_mfma_f32_16x16x32_bf16(__builtin_bit_cast(bf16x8, a), __builtin_bit_cast(bf16x8, b), c, 0, 0, 0);
}
__device__ __forceinline__ u32 pk(float lo, float hi) {
  bf16x2 v = {(__bf16)lo, (__bf16)hi};
  return __builtin_bit_cast(u32, v);
}
__device__ __forceinline__ u32 san(u32 v) { return v < 0xFFFFFFFEu ? v : 0xFFFFFFFEu; }
__device__ __forceinline__ u32 relu2(u32 v) {
  s16x2 a = __builtin_bit_cast(s16x2, v), z = {0, 0};
  return __builtin_bit_cast(u32, __builtin_elementwise_max(a, z));
}
__device__ __forceinline__ float bflo(u32 v) { return __uint_as_float(v << 16); }
__device__ __forceinline__ float bfhi(u32 v) { return __uint_as_float(v & 0xffff0000u); }
__device__ __forceinline__ void glds16(const char* base, u32 voff, u32 lds) {
  u32 sv;
  asm volatile("s_mov_b32 %0, m0\n\ts_mov_b32 m0, %3\n\ts_nop 0\n\tglobal_load_lds_dwordx4 %1, %2\n\ts_mov_b32 m0, %0"
               : "=&s"(sv) : "v"(voff), "s"(base), "s"(lds) : "memory");
}
__device__ __forceinline__ void glds16s(const char* base, u32 voff, u32 lds) {
  u32 sv;
  asm volatile("s_mov_b32 %0, m0\n\ts_mov_b32 m0, %3\n\ts_nop 0\n\tglobal_load_lds_dwordx4 %1, %2 sc1\n\ts_mov_b32 m0, %0"
               : "=&s"(sv) : "v"(voff), "s"(base), "s"(lds) : "memory");
}
__device__ __forceinline__ void glds4(const char* base, u32 voff, u32 lds) {
  u32 sv;
  asm volatile("s_mov_b32 %0, m0\n\ts_mov_b32 m0, %3\n\ts_nop 0\n\tglobal_load_lds_dword %1, %2\n\ts_mov_b32 m0, %0"
               : "=&s"(sv) : "v"(voff), "s"(base), "s"(lds) : "memory");
}
template <int N> __device__ __forceinline__ void waitv() { asm volatile("s_waitcnt vmcnt(%0)" ::"n"(N) : "memory"); }
#define WAITV(n) asm volatile("s_waitcnt vmcnt(" #n ")" ::: "memory")
#define BAR() asm volatile("s_waitcnt lgkmcnt(0)\n\ts_barrier" ::: "memory")
#define SB() __builtin_amdgcn_sched_barrier(0)
__device__ __forceinline__ u32x2 trread(u32 addr) {
  s16x4 v = __builtin_amdgcn_ds_read_tr16_b64_v4i16((LAS s16x4*)addr);
  return __builtin_bit_cast(u32x2, v);
}
__device__ __forceinline__ u32x4 trfrag(u32 a0, u32 a1) {
  u32x2 lo = trread(a0), hi = trread(a1);
  return u32x4{lo.x, lo.y, hi.x, hi.y};
}
__device__ __forceinline__ u32x4 lds128(u32 a) { return *(LAS u32x4*)a; }
__device__ __forceinline__ f32x2 ldsf2(u32 a) { return *(LAS f32x2*)a; }
__device__ __forceinline__ f32x4 ldsf4(u32 a) { return *(LAS f32x4*)a; }
__device__ __forceinline__ void sts128(u32 a, u32x4 v) { *(LAS u32x4*)a = v; }
__device__ __forceinline__ void sts64(u32 a, u32x2 v) { *(LAS u32x2*)a = v; }
__device__ __forceinline__ void stsf2(u32 a, f32x2 v) { *(LAS f32x2*)a = v; }
__device__ __forceinline__ void stsf4(u32 a, f32x4 v) { *(LAS f32x4*)a = v; }
__device__ __forceinline__ float ldsf(u32 a) { return *(LAS float*)a; }
__device__ __forceinline__ float rows_sum(float v) {
  auto a = __builtin_amdgcn_permlane16_swap(__float_as_uint(v), __float_as_uint(v), false, false);
  v = __uint_as_float(a[0]) + __uint_as_float(a[1]);
  auto c = __builtin_amdgcn_permlane32_swap(__float_as_uint(v), __float_as_uint(v), false, false);
  return __uint_as_float(c[0]) + __uint_as_float(c[1]);
}
__device__ __forceinline__ float rows_max(float v) {
  auto a = __builtin_amdgcn_permlane16_swap(__float_as_uint(v), __float_as_uint(v), false, false);
  v = fmaxf(__uint_as_float(a[0]), __uint_as_float(a[1]));
  auto c = __builtin_amdgcn_permlane32_swap(__float_as_uint(v), __float_as_uint(v), false, false);
  return fmaxf(__uint_as_float(c[0]), __uint_as_float(c[1]));
}
template <int CTRL> __device__ __forceinline__ float dppf(float v) {
  return __uint_as_float(__builtin_amdgcn_update_dpp(0u, __float_as_uint(v), CTRL, 0xf, 0xf, false));
}
__device__ __forceinline__ float row_sum(float v) {
  v += dppf<0x128>(v); v += dppf<0x124>(v); v += dppf<0x122>(v); v += dppf<0x121>(v);
  return v;
}
__device__ __forceinline__ float dot2(u32 a, u32 b, float c) {
  return __builtin_amdgcn_fdot2_f32_bf16(__builtin_bit_cast(bf16x2, a), __builtin_bit_cast(bf16x2, b), c, false);
}
__device__ __forceinline__ float wave_sum(float v) {
  v += dppf<0x128>(v); v += dppf<0x124>(v); v += dppf<0x122>(v); v += dppf<0x121>(v);
  return rows_sum(v);
}
#define PIN(x) asm volatile("" : "+v"(x))
__device__ __forceinline__ u32x4 ald128(const char* base, u32 voff) {
  u32x4 v; asm volatile("global_load_dwordx4 %0, %1, %2" : "=v"(v) : "v"(voff), "s"(base) : "memory"); return v;
}
__device__ __forceinline__ u32x4 ald128s(const char* base, u32 voff) {
  u32x4 v; asm volatile("global_load_dwordx4 %0, %1, %2 sc1" : "=v"(v) : "v"(voff), "s"(base) : "memory"); return v;
}
__device__ __forceinline__ u32x2 ald64s(const char* base, u32 voff) {
  u32x2 v; asm volatile("global_load_dwordx2 %0, %1, %2 sc1" : "=v"(v) : "v"(voff), "s"(base) : "memory"); return v;
}
__device__ __forceinline__ float bperm(float v, int srclane) {
  return __uint_as_float(__builtin_amdgcn_ds_bpermute(srclane * 4, __float_as_uint(v)));
}
__device__ __forceinline__ u32 umax3(u32 a, u32 b, u32 c) { return max(max(a, b), c); }
__device__ __forceinline__ u32 chk(u32 bad, u32x4 v) { return umax3(umax3(bad, v.x, v.y), v.z, v.w); }

__device__ __forceinline__ void wait_flags(const char* ws, u32 off, u64 tag) {
  gu64* p = (gu64*)(ws + off);
#pragma nounroll
  for (int it = 0; it < SPIN_CAP; ++it) {
    u64 v = __hip_atomic_load(p, RLX_AGENT);
    if (__all(v == tag)) break;
    __builtin_amdgcn_s_sleep(2);
  }
  asm volatile("" ::: "memory");
}
__device__ __forceinline__ void set_flag(char* ws, u32 off, u64 tag, int lane) {
  if (lane == 0) __hip_atomic_store((gu64*)(ws + off), tag, RLX_AGENT);
}
#define LD128(off) __builtin_amdgcn_raw_buffer_load_b128(rs, (off), 0, 16)
#define LD64(off) __builtin_amdgcn_raw_buffer_load_b64(rs, (off), 0, 16)
#define ST128(v, off) __builtin_amdgcn_raw_buffer_store_b128((v), rs, (off), 0, 16)
#define ST64(v, off) __builtin_amdgcn_raw_buffer_store_b64((v), rs, (off), 0, 16)
#define LDL128(soff) __builtin_amdgcn_raw_buffer_load_b128(rs, l16, (soff), 16)
#define LDL64(soff) __builtin_amdgcn_raw_buffer_load_b64(rs, l8, (soff), 16)
#define STL128(v, soff) __builtin_amdgcn_raw_buffer_store_b128((v), rs, l16 + (soff), 0, 16)
#define STL64(v, soff) __builtin_amdgcn_raw_buffer_store_b64((v), rs, l8, (soff), 16)
__device__ __forceinline__ void canary(const __amdgpu_buffer_rsrc_t rs, u32 off) {
#pragma nounroll
  for (int it = 0; it < SPIN_CAP; ++it) {
    asm volatile("" ::: "memory");
    const u32x4 v = LD128(off);
    if (__all(chk(0, v) != SENT)) break;
  }
  asm volatile("" ::: "memory");
}

__device__ __forceinline__ void sig_nw(u32 addr) {
  asm volatile("s_mov_b64 exec, 1\n\tds_add_u32 %0, %1\n\ts_mov_b64 exec, -1" ::"v"(addr), "v"(1u) : "memory");
}
__device__ __forceinline__ void sig(u32 addr) {
  asm volatile("s_waitcnt lgkmcnt(0)\n\ts_mov_b64 exec, 1\n\tds_add_u32 %0, %1\n\ts_mov_b64 exec, -1" ::"v"(addr), "v"(1u) : "memory");
}
template <int N> __device__ __forceinline__ void await(u32 addr) {
  u32 v, sv, it;
  asm volatile(
      "s_mov_b32 %2, 0x100000\n"
      "1:\n\t"
      "ds_read_b32 %0, %3\n\t"
      "s_waitcnt lgkmcnt(0)\n\t"
      "v_readfirstlane_b32 %1, %0\n\t"
      "s_sub_u32 %2, %2, 1\n\t"
      "s_cselect_b32 %1, %4, %1\n\t"
      "s_cmp_lt_u32 %1, %4\n\t"
      "s_cbranch_scc1 1b"
      : "=&v"(v), "=&s"(sv), "=&s"(it) : "v"(addr), "n"(N) : "memory", "scc");
}
__device__ __forceinline__ u32 peek(u32 addr) {
  return __hip_atomic_load((LAS u32*)addr, __ATOMIC_RELAXED, __HIP_MEMORY_SCOPE_WORKGROUP);
}
template <int N, int L, class RD, class MM>
__device__ __forceinline__ void pipe(RD rd, MM mm) {
  u32x4 ring[L + 1];
#pragma unroll
  for (int j = 0; j < L; ++j) if (j < N) ring[j] = rd(j);
  SB();
#pragma unroll
  for (int j = 0; j < N; ++j) {
    if (j + L < N) ring[(j + L) % (L + 1)] = rd(j + L);
    mm(j, ring[j % (L + 1)]);
    SB();
  }
}
#define STAMPP(k)
#define STAMP(k) do {} while (0)

__global__ void __launch_bounds__(512, 2) fused_block(Params p) {
  __shared__ __attribute__((aligned(16))) char smem[163840];
  STAMP(0);
  u64 pc; u32 len;
  asm volatile("s_getpc_b64 %0\n.Lfb_code_begin:\n\ts_mov_b32 %1, .Lfb_end_a-.Lfb_code_begin\n\ts_max_u32 %1, %1, .Lfb_end_b-.Lfb_code_begin\n\ts_max_u32 %1, %1, .Lfb_end_c-.Lfb_code_begin" : "=s"(pc), "=s"(len) :: "scc");
  const u32 L0 = (u32)(uintptr_t)(LAS char*)smem;
  const int tid = threadIdx.x, lane = tid & 63;
  const int w = __builtin_amdgcn_readfirstlane(tid >> 6);
  if (tid < 32) *(LAS u32*)(L0 + L_CNT + tid * 4) = 0;
  BAR();
  const int g4 = lane >> 4, i16 = lane & 15, q4 = (lane >> 2) & 3, p4 = lane & 3;
  const int b = blockIdx.x, grp = b & 7, idx = b >> 3;
  const int rb = 2 * grp + (idx >> 4), hc = idx & 15;
  const bool act3 = hc < 14;
  const int q1 = hc >= 7 ? 1 : 0, nt = hc >= 14 ? hc - 14 : hc - 7 * q1;
  const int mp = w & 3, hsel = w >> 2, kind = hc & 1;
  const int mt = 2 * mp + hsel;
  const bool team = w >= 4;
  const u32 l16 = lane * 16, l8 = lane * 8;
  char* ws = p.ws;
  const u32 RB = (u32)rb * RBS;
  const __amdgpu_buffer_rsrc_t rs = __builtin_amdgcn_make_buffer_rsrc(ws, 0, WS_END, 0x00020000);
  const int step = 1 + rb % 6;
  auto chunk_of = [&](int c) { return (nt + c * step) % 7; };
  const u32 CNT = L0 + L_CNT;
  u32x4 ones = {0x3F803F80u, 0x3F803F80u, 0x3F803F80u, 0x3F803F80u};
  const u32 wsh = (hc & 1) * 8;
  u32x2 xr[4];
  auto offw = [&](int hs, int t) {
    return L0 + L_WKV + (8 * g4 + q4) * 128 + ((56 * hs + 32 * t + 8 * p4) ^ (32 * (((q4 >> 1) & 1) | ((g4 & 1) << 1))));
  };
  auto offq = [&](int t) { return L0 + L_WQ + (8 * g4 + q4) * 64 + (((wsh + 8 * (4 * t + p4)) & 63) ^ (32 * (g4 & 1))); };
  auto get_tag = [&]() {
    const u32x2 tg = *(LAS u32x2*)(L0 + L_TAG);
    return ((u64)(u32)__builtin_amdgcn_readfirstlane(tg.y) << 32) | (u32)__builtin_amdgcn_readfirstlane(tg.x);
  };
  u32x4 qf;
  u64 tag;

  if (__builtin_expect(team, 1)) {
    const int v = w & 1;
    if (__builtin_expect(w < 6, 1)) {
      const u32 xsrc = (u32)(128 * rb + 64 * v + (lane >> 3)) * 896u;
      const u32 xs0 = xsrc + (((lane & 7) ^ (lane >> 4)) * 16), xs1 = xsrc + 8 * 896 + (((lane & 7) ^ (4 + (lane >> 4))) * 16);
      int wc0 = (lane & 7) ^ (2 * ((lane >> 4) & 1)), wc1 = wc0 ^ 4;
      wc0 = wc0 > 6 ? 6 : wc0; wc1 = wc1 > 6 ? 6 : wc1;
      const u32 wrow = (u32)(32 * v + (lane >> 3)) * 2688u + (kind + 1) * 896 + (hc & ~1) * 56;
      const u32 ws0 = wrow + wc0 * 16, ws1 = wrow + 8 * 2688 + wc1 * 16;
      auto issue_chunk = [&](int c) {
        const int cc = chunk_of(c);
        const u32 sl = L0 + (c == 6 ? 0 : c) * SLOT;
        const char* xb = p.x + cc * 128;
        const char* wb = p.wqkv + (size_t)(64 * cc * 2688);
#pragma unroll
        for (int k = 0; k < 4; ++k) {
          glds16(xb + k * 16 * 896, xs0, sl + v * 8192 + k * 2048);
          glds16(xb + k * 16 * 896, xs1, sl + v * 8192 + k * 2048 + 1024);
          glds16(wb + (k >> 1) * 16 * 2688, (k & 1) ? ws1 : ws0, L0 + L_WKV + c * 8192 + v * 4096 + k * 1024);
        }
      };
      __builtin_amdgcn_s_setprio(2);
      issue_chunk(0);
      issue_chunk(1);
      waitv<12>(); sig_nw(CNT + (C_CH + 0) * 4);
      issue_chunk(2);
#pragma nounroll
      for (int c = 3; c < 7; ++c) {
        if (c == 3) { waitv<12>(); sig_nw(CNT + (C_CH + 1) * 4); }
        if (c == 6) await<4>(CNT + (C_RD + 0) * 4);
        issue_chunk(c);
        if (c == 3) { waitv<12>(); sig_nw(CNT + (C_CH + 2) * 4); }
        else if (c > 4) { waitv<24>(); sig_nw(CNT + (C_CH + c - 2) * 4); }
      }
      STAMPP(8);
      const u32 qsrc = (u32)(lane >> 2) * 2688u + hc * 56 - wsh + (((lane & 3) ^ (2 * ((lane >> 5) & 1))) * 16);
      auto issue_wq = [&](int k) {
        const int cc = chunk_of(k);
        glds16(p.wqkv + (size_t)((64 * cc + 32 * v) * 2688), qsrc, L0 + L_WQ + (4 * k + 2 * v) * 1024);
        glds16(p.wqkv + (size_t)((64 * cc + 32 * v + 16) * 2688), qsrc, L0 + L_WQ + (4 * k + 2 * v + 1) * 1024);
      };
      waitv<12>(); sig_nw(CNT + (C_CH + 5) * 4);
      await<4>(CNT + (C_RD + 2) * 4);
      issue_wq(0); issue_wq(1);
      waitv<4>();  sig_nw(CNT + (C_CH + 6) * 4);
      STAMPP(9);
#pragma nounroll
      for (int k = 2; k < 7; ++k) issue_wq(k);
      waitv<0>();  sig_nw(CNT + C_WQ * 4);
      __builtin_amdgcn_s_setprio(0);
      await<2>(CNT + C_CS * 4);
      tag = get_tag();
    } else {
      const u64 epoch = __hip_atomic_load((gu64*)(ws + WS_CTL), RLX_AGENT);
      __builtin_amdgcn_s_setprio(3);
      u32x4 xrow[4];
#pragma unroll
      for (int k = 0; k < 4; ++k)
        xrow[k] = *(const GAS u32x4*)(p.x + (size_t)(128 * rb + 8 * hc + 4 * v + g4) * 896 + (k < 3 ? i16 + 16 * k : 48 + (i16 & 7)) * 16);
      {
        u32 off = (u32)(((idx & 7) * 2 + v) * 2816 + (lane < 44 ? lane : 0) * 64);
        off = off < len ? off : 0;
        glds4((const char*)pc, off, L0 + L_DUMMY);
      }
      {
        u64 e = ((u64)(u32)__builtin_amdgcn_readfirstlane((u32)(epoch >> 32)) << 32) | (u32)__builtin_amdgcn_readfirstlane((u32)epoch);
        e += 0x9E3779B97F4A7C15ull;
        e = (e ^ (e >> 30)) * 0xBF58476D1CE4E5B9ull;
        e = (e ^ (e >> 27)) * 0x94D049BB133111EBull;
        tag = e ^ (e >> 31);
        if (lane == 0) sts64(L0 + L_TAG, u32x2{(u32)tag, (u32)(tag >> 32)});
      }
      {
        if (i16 >= 8) xrow[3] = u32x4{0, 0, 0, 0};
        float s1 = 0.f, s2 = 0.f, s1b = 0.f, s2b = 0.f;
#pragma unroll
        for (int k = 0; k < 4; ++k) {
          s1 = dot2(xrow[k][0], 0x3F803F80u, s1); s2 = dot2(xrow[k][0], xrow[k][0], s2);
          s1b = dot2(xrow[k][1], 0x3F803F80u, s1b); s2b = dot2(xrow[k][1], xrow[k][1], s2b);
          s1 = dot2(xrow[k][2], 0x3F803F80u, s1); s2 = dot2(xrow[k][2], xrow[k][2], s2);
          s1b = dot2(xrow[k][3], 0x3F803F80u, s1b); s2b = dot2(xrow[k][3], xrow[k][3], s2b);
        }
        s1 = row_sum(s1 + s1b); s2 = row_sum(s2 + s2b);
        const float mean = s1 * (1.0f / D);
        const float rstd = __builtin_amdgcn_rsqf(fmaxf(s2 * (1.0f / D) - mean * mean, 0.f) + 1e-5f);
        ST128((u32x4{__float_as_uint(mean), (u32)tag, __float_as_uint(rstd), (u32)(tag >> 32)}),
              i16 == 0 ? RB + B_G + (u32)(8 * hc + 4 * v + g4) * 16u : WS_OOB);
      }
      SB();
      STAMPP(5);
      const u32 cw[2] = {offw(v, 0), offw(v, 1)};
      f32x4 cs[2] = {{0, 0, 0, 0}, {0, 0, 0, 0}};
      u32x4 gv;
      const u32 gof = RB + B_G + (u32)(64 * v + lane) * 16u;
      auto cs_chunk = [&](int c) {
        await<2>(CNT + (C_CH + c) * 4);
        u32x4 f[4];
#pragma unroll
        for (int z = 0; z < 4; ++z) { const u32 a = cw[z & 1] + c * 8192 + (z >> 1) * 4096; f[z] = trfrag(a, a + 512); }
#pragma unroll
        for (int z = 0; z < 4; ++z) cs[z & 1] = mfma(f[z], ones, cs[z & 1]);
        SB();
      };
#pragma nounroll
      for (int c = 0; c < 4; ++c) cs_chunk(c);
      gv = LD128(gof);
      SB();
#pragma nounroll
      for (int c = 4; c < 6; ++c) cs_chunk(c);
      if (__builtin_expect(!__all(gv.y == (u32)tag && gv.w == (u32)(tag >> 32)), 0)) {
#pragma nounroll
        for (int it = 0; it < SPIN_CAP; ++it) {
          asm volatile("" ::: "memory");
          gv = LD128(gof);
          if (__all(gv.y == (u32)tag && gv.w == (u32)(tag >> 32))) break;
        }
      }
      stsf2(L0 + L_SX + (64 * v + lane) * 8, f32x2{__uint_as_float(gv.x), __uint_as_float(gv.z)});
      STAMPP(6);
      cs_chunk(6);
      if (i16 == 0) { stsf4(L0 + L_CS + v * 128 + g4 * 16, cs[0]); stsf4(L0 + L_CS + v * 128 + 64 + g4 * 16, cs[1]); }
      sig(CNT + C_CS * 4);
      STAMPP(7);
      __builtin_amdgcn_s_setprio(0);
      {
        await<2>(CNT + C_WQ * 4);
        const u32 a = offq(v);
        f32x4 cq = {0, 0, 0, 0};
        pipe<14, 6>([&](int j) { return trfrag(a + j * 2048, a + j * 2048 + 256); }, [&](int j, u32x4 f) { cq = mfma(f, ones, cq); });
        if (i16 == 0) stsf4(L0 + L_CQ + v * 64 + g4 * 16, cq);
        sig(CNT + C_CQ * 4);
      }
      asm volatile(".Lfb_end_c:" ::: "memory");
    }
    STAMP(4);
  } else {
    const u32 xa0 = L0 + mp * 4096 + (i16 >> 3) * 1024 + (i16 & 7) * 128;
    u32 xo[2] = {xa0 + ((g4 ^ ((i16 >> 1) & 7)) * 16), xa0 + (((4 + g4) ^ ((i16 >> 1) & 7)) * 16)};
    u32 offW[4] = {offw(0, 0), offw(0, 1), offw(1, 0), offw(1, 1)};
    u32 qa[2] = {offq(0), offq(1)};
    u32 xo2[2] = {xo[0] + 3 * SLOT, xo[1] + 3 * SLOT};
    u32 xra[4];
#pragma unroll
    for (int t = 0; t < 4; ++t) {
      xra[t] = L0 + (4 * q1 + w) * 2048 + (i16 >> 3) * 1024 + (i16 & 7) * 128 + (g4 & 1) * 8 + (((2 * t + (g4 >> 1)) ^ ((i16 >> 1) & 7)) * 16);
      PIN(xra[t]);
    }
    PIN(xo2[0]); PIN(xo2[1]);
    PIN(xo[0]); PIN(xo[1]); PIN(offW[0]); PIN(offW[1]); PIN(offW[2]); PIN(offW[3]); PIN(qa[0]); PIN(qa[1]);
    SB();
    if (w == 0) { if (b < 147) glds4(p.wqkv + b * 8192, lane * 128, L0 + L_DUMMY); }
    else if (w == 2) glds4(ws + (size_t)RB, i16 * 65536 + hc * 4096 + 2048, L0 + L_DUMMY);
    else if (w == 3) glds4(p.out + (size_t)rb * 114688, (i16 < 14 ? i16 : 0) * 8192 + hc * 512, L0 + L_DUMMY);
    auto warm = [&]() {
      const int u = 2 * b + w;
      if (w < 2 && u < 441) {
        const char* base = u < 196 ? p.w1 + u * 8192 : u < 392 ? p.w2 + (u - 196) * 8192 : p.wo + (u - 392) * 8192;
        glds4(base, lane * 128, L0 + L_DUMMY);
      }
    };
    u32x4 xf[7][2][2];
    auto p1_loop = [&](auto KIND) {
      constexpr int KD = decltype(KIND)::value;
      f32x4 acc1[2][4];
#pragma unroll
      for (int t = 0; t < 4; ++t) acc1[0][t] = acc1[1][t] = f32x4{0, 0, 0, 0};
      u32x2 bh[2][4][2];
      u32 cv = 0;
      auto rd = [&](int j, int z) {
        const int sn = (j >> 1) % 6;
        const u32 sl = (sn % 3) * SLOT;
        if (z < 2) xf[j >> 1][j & 1][z] = lds128((sn < 3 ? xo[j & 1] : xo2[j & 1]) + sl + z * 2048);
        else bh[j & 1][(z - 2) >> 1][z & 1] = trread(offW[(z - 2) >> 1] + j * 4096 + (z & 1) * 512);
      };
      auto mf = [&](int j, int z) {
        const int t = z >> 1, a = z & 1;
        const u32x4 bw = {bh[j & 1][t][0].x, bh[j & 1][t][0].y, bh[j & 1][t][1].x, bh[j & 1][t][1].y};
        acc1[a][t] = KD == 0 ? mfma(bw, xf[j >> 1][j & 1][a], acc1[a][t]) : mfma(xf[j >> 1][j & 1][a], bw, acc1[a][t]);
      };
      await<2>(CNT + (C_CH + 0) * 4);
      STAMP(1);
#pragma unroll
      for (int z = 0; z < 10; ++z) rd(0, z);
#pragma unroll
      for (int t = 0; t < 4; ++t) xr[t] = *(LAS u32x2*)xra[t];
      SB();
#pragma unroll
      for (int j = 0; j < 14; ++j) {
        const bool edge = (j & 1) && j < 13;
        if (edge && j != 11) {
          if (__builtin_expect((u32)__builtin_amdgcn_readfirstlane(cv) < 2u, 0)) await<2>(CNT + (C_CH + (j + 1) / 2) * 4);
        }
        if (!(j & 1) && j < 10) cv = peek(CNT + (C_CH + j / 2 + 1) * 4);
        if (j == 6) STAMPP(6);
        if (j == WARM_J) warm();
#pragma unroll
        for (int z = 0; z < 8; ++z) {
          if (j < 13 && j != 11) { rd(j + 1, z < 2 ? 2 * z : z + 2); if (z < 2) rd(j + 1, 2 * z + 1); }
          mf(j, z);
          SB();
        }
        if (j == 1 || j == 5 || j == 11) sig_nw(CNT + (C_RD + (j - 1) / 2) * 4);
        if (j == 11) {
          STAMPP(10);
          await<2>(CNT + (C_CH + 6) * 4);
          STAMPP(11);
#pragma unroll
          for (int z = 0; z < 10; ++z) rd(12, z);
          SB();
        }
      }
      sig_nw(CNT + (C_RD + 6) * 4);
      await<2>(CNT + C_CS * 4);
      await<4>(CNT + (C_RD + 5) * 4);
      const u32 go = RB + B_KV + (u32)((hc & ~1) * 4 + mp) * 4096u + KD * 2048;
      const u32 lo = L0 + L_KV + (8 + mp) * 4096 + KD * 2048 + lane * 16;
      if (KD == 0) {
        f32x2 ms[2]; f32x4 cs[4];
#pragma unroll
        for (int a = 0; a < 2; ++a) ms[a] = ldsf2(L0 + L_SX + ((2 * mp + a) * 16 + i16) * 8);
#pragma unroll
        for (int t = 0; t < 4; ++t) cs[t] = ldsf4(L0 + L_CS + t * 64 + g4 * 16);
#pragma unroll
        for (int hs = 0; hs < 2; ++hs) {
#pragma unroll
          for (int a = 0; a < 2; ++a) {
            u32x4 kf;
#pragma unroll
            for (int t = 0; t < 2; ++t) {
              float kv[4];
#pragma unroll
              for (int r = 0; r < 4; ++r) kv[r] = ms[a].y * (acc1[a][2 * hs + t][r] - ms[a].x * cs[2 * hs + t][r]);
              kf[2 * t] = pk(kv[0], kv[1]); kf[2 * t + 1] = pk(kv[2], kv[3]);
            }
            if (g4 == 3) { kf[2] = 0; kf[3] = 0; }
            STL128(kf, go + hs * 4 * 4096 + a * 1024);
            if (hs == KD) sts128(lo + a * 1024, kf);
          }
        }
      } else {
        f32x4 ms[2][2]; float cs[4];
#pragma unroll
        for (int a = 0; a < 2; ++a) {
          ms[a][0] = ldsf4(L0 + L_SX + ((2 * mp + a) * 16 + 4 * g4) * 8);
          ms[a][1] = ldsf4(L0 + L_SX + ((2 * mp + a) * 16 + 4 * g4) * 8 + 16);
        }
#pragma unroll
        for (int t = 0; t < 4; ++t) cs[t] = ldsf(L0 + L_CS + t * 64 + i16 * 4);
#pragma unroll
        for (int hs = 0; hs < 2; ++hs)
#pragma unroll
          for (int t = 0; t < 2; ++t) {
            u32x4 vf;
#pragma unroll
            for (int a = 0; a < 2; ++a) {
              float vv[4];
#pragma unroll
              for (int r = 0; r < 4; ++r)
                vv[r] = ms[a][r >> 1][2 * (r & 1) + 1] * (acc1[a][2 * hs + t][r] - ms[a][r >> 1][2 * (r & 1)] * cs[2 * hs + t]);
              vf[2 * a] = pk(vv[0], vv[1]); vf[2 * a + 1] = pk(vv[2], vv[3]);
            }
            if (t == 1 && i16 >= 12) vf = (i16 == 12) ? ones : u32x4{0, 0, 0, 0};
            STL128(vf, go + hs * 4 * 4096 + t * 1024);
            if (hs == KD) sts128(lo + t * 1024, vf);
          }
      }
    };
    if (kind == 0) p1_loop(std::integral_constant<int, 0>{}); else p1_loop(std::integral_constant<int, 1>{});
    SB();
    sig_nw(CNT + C_KV * 4);
    STAMP(2);
    tag = get_tag();
    await<2>(CNT + C_WQ * 4);
    STAMP(3);
    {
      const u32x4 sent4 = {SENT, SENT, SENT, SENT};
      f32x4 aq[2][2] = {{{0, 0, 0, 0}, {0, 0, 0, 0}}, {{0, 0, 0, 0}, {0, 0, 0, 0}}};
      pipe<28, 4>([&](int j) { const u32 a = qa[j & 1] + (j >> 1) * 2048; return trfrag(a, a + 256); },
                  [&](int j, u32x4 f) {
                    aq[0][j & 1] = mfma(f, xf[j >> 2][(j >> 1) & 1][0], aq[0][j & 1]);
                    aq[1][j & 1] = mfma(f, xf[j >> 2][(j >> 1) & 1][1], aq[1][j & 1]);
                    if (j == 14) {
                      WAITV(0);
                      STAMPP(7);
                      set_flag(ws, RB + B_F1 + (u32)(((hc >> 1) * 4 + mp) * 2 + kind) * FSTR, tag, lane);
                    }
                    if (j == 16 || j == 18) STL128(sent4, RB + B_O + (u32)((2 * mp + ((j >> 1) & 1)) * 16 + hc) * 1024u);
                    if (j == 20 || j == 22) ST128(sent4, act3 ? RB + B_HH + (u32)((4 * q1 + w) * 14 + 2 * nt + ((j >> 1) & 1)) * 1024u + l16 : WS_OOB);
                    if (j == 24) ST64((u32x2{SENT, SENT}), (act3 && lane < 16) ? RB + B_ST + (u32)((4 * q1 + w) * 8 + nt) * 128u + l8 : WS_OOB);
                  });
      await<2>(CNT + C_CQ * 4);
      await<4>(CNT + (C_RD + 6) * 4);
      f32x4 cq[2];
#pragma unroll
      for (int t = 0; t < 2; ++t) cq[t] = ldsf4(L0 + L_CQ + t * 64 + g4 * 16);
#pragma unroll
      for (int a = 0; a < 2; ++a) {
        const f32x2 ms = ldsf2(L0 + L_SX + ((2 * mp + a) * 16 + i16) * 8);
        const float qs = ms.y * (0.18898223650461362f * 1.4426950408889634f);
        u32x4 q;
#pragma unroll
        for (int t = 0; t < 2; ++t) {
          float qv[4];
#pragma unroll
          for (int r = 0; r < 4; ++r) qv[r] = qs * (aq[a][t][r] - ms.x * cq[t][r]);
          q[2 * t] = pk(qv[0], qv[1]); q[2 * t + 1] = pk(qv[2], qv[3]);
        }
        if (g4 == 3) { q[2] = 0x00003F80u; q[3] = 0; }
        if (a == 0) qf = q; else sts128(L0 + L_Q + mp * 1024 + lane * 16, q);
      }
    }
    STAMP(4);
    WAITV(0);
    asm volatile(".Lfb_end_b:" ::: "memory");
  }
  f32x4 mk[4];
#pragma unroll
  for (int z = 0; z < 4; ++z) {
    const int n = z < 2 ? z : 14 + z;
#pragma unroll
    for (int r = 0; r < 4; ++r) {
      const int dist = 16 * (16 + hsel - n) + i16 - 4 * g4 - r;
      mk[z][r] = ((u32)dist < 256u) ? 0.f : -1e30f;
    }
    PIN(mk[z]);
  }
  const int krot = 7 * (rb & 1);
  u32 w1src[11];
#pragma unroll
  for (int k = 0; k < 11; ++k) {
    int e = mp + 4 * k; e = e > 41 ? 41 : e;
    const int ch = (7 + e + 7 * krot) * 64 + lane, row = ch / 14, cc = ch - row * 14;
    w1src[k] = (u32)row * 3584u + hc * 224 + cc * 16;
    PIN(w1src[k]);
  }
  const int kr = w ^ (4 * q1);
  const int e8 = (lane >> 3) & 1;
  u32 w2o = act3 ? (u32)(224 * kr + 4 * g4 + 32 * e8) * 896u + nt * 128 + (lane & 7) * 16 : WS_OOB;
  u32 w2o3 = act3 ? (u32)(224 * kr + 4 * g4 + 192 + 16 * e8) * 896u + nt * 128 + (lane & 7) * 16 : WS_OOB;
  u32 osrc = (u32)(lane >> 3) * 896u + nt * 128 + ((((lane & 7) >> 1) ^ ((lane >> 4) & 3)) * 32) + (lane & 1) * 16;
  PIN(w2o); PIN(w2o3); PIN(osrc);
  u32x4 raw[28];
  const __amdgpu_buffer_rsrc_t rw2 = __builtin_amdgcn_make_buffer_rsrc((void*)p.w2, 0, DFF * D * 2, 0x00020000);
  auto load_w2 = [&](int n) {
    if (n < 24) raw[n] = __builtin_amdgcn_raw_buffer_load_b128(rw2, w2o + (n & 3) * 896, (64 * (n >> 3) + 16 * ((n & 7) >> 2)) * 896, 0);
    else raw[n] = __builtin_amdgcn_raw_buffer_load_b128(rw2, w2o3 + (n - 24) * 896, 0, 0);
  };
  constexpr int EW2 = 14;
  if (team) {
    await<4>(CNT + C_KV * 4);
#pragma unroll
    for (int n = 0; n < EW2; ++n) load_w2(n);
    SB();
    const int hrb = rb - 2 + (mp >> 1);
    const u32 fpart = RB + B_F1 + (u32)(((hc >> 1) * 4 + mp) * 2 + (kind ^ 1)) * FSTR;
    const u32 opart = RB + B_KV + (u32)(hc * 4 + mp) * 4096u + (kind ^ 1) * 2048;
    if (hrb >= 0) {
      wait_flags(ws, (lane & 4) ? fpart : (u32)hrb * RBS + B_F1 + (u32)(((hc >> 1) * 4 + 2 * (mp & 1)) * 2 + (lane & 3)) * FSTR, tag);
      STAMPP(11);
      const char* o = ws + (size_t)((u32)hrb * RBS + B_KV + (u32)(hc * 4 + 2 * (mp & 1)) * 4096u);
#pragma unroll
      for (int k = 0; k < 8; ++k) glds16s(o + k * 1024, l16, L0 + L_KV + mp * 8192 + k * 1024);
    } else {
      const u32 neg = (g4 == 3) ? 0x0000F149u : 0u;
#pragma unroll
      for (int k = 0; k < 8; ++k) sts128(L0 + L_KV + mp * 8192 + k * 1024 + lane * 16, (k & 2) ? u32x4{0, 0, 0, 0} : u32x4{0, 0, neg, 0});
      wait_flags(ws, fpart, tag);
    }
    glds16s(ws + (size_t)opart, l16, L0 + L_KV + (8 + mp) * 4096 + (kind ^ 1) * 2048);
    glds16s(ws + (size_t)opart + 1024, l16, L0 + L_KV + (8 + mp) * 4096 + (kind ^ 1) * 2048 + 1024);
    STAMPP(10);
    WAITV(0);
  }
  BAR();
  STAMP(5);
  auto attention = [&](auto TEAM) {
  constexpr bool TM = decltype(TEAM)::value;
  auto bg = [&](int z) {
    if (TM) {
      if (z < 14) glds16(p.wo + (size_t)((mp + 4 * z) * 8 * 896), osrc, L0 + L_WO + (mp + 4 * z) * 1024);
      else { int e = mp + 4 * (z - 14); e = e > 41 ? 41 : e; glds16(p.w1, w1src[z - 14], L0 + L_W1 + (7 + e) * 1024); }
    } else if (z >= 7 && z - 7 < EW2) load_w2(z - 7);
  };
  {
    if (TM) qf = lds128(L0 + L_Q + mp * 1024 + lane * 16);
    if (TM && w == 7) set_flag(ws, RB + B_R1 + (u32)hc * FSTR, tag, lane);
    const u32 kb = L0 + L_KV + mp * 4096 + lane * 16;
    f32x4 sc[18];
    float m0 = -3e38f, m1 = -3e38f;
    pipe<18, 5>([&](int n) { return lds128(kb + (n >> 1) * 4096 + (n & 1) * 1024); },
                [&](int n, u32x4 f) {
                  const f32x4 c0 = n < 2 ? mk[n] : n >= 16 ? mk[n - 14] : f32x4{0, 0, 0, 0};
                  sc[n] = mfma(f, qf, c0);
                  if (n >= 2) { m0 = fmaxf(fmaxf(m0, sc[n - 2][0]), sc[n - 2][1]); m1 = fmaxf(fmaxf(m1, sc[n - 2][2]), sc[n - 2][3]); }
                  if ((n & 1) == 1 && n < 14) bg(n >> 1);
                });
#pragma unroll
    for (int n = 16; n < 18; ++n) { m0 = fmaxf(fmaxf(m0, sc[n][0]), sc[n][1]); m1 = fmaxf(fmaxf(m1, sc[n][2]), sc[n][3]); }
    const float m = rows_max(fmaxf(m0, m1));
    f32x4 oa[2] = {{0, 0, 0, 0}, {0, 0, 0, 0}};
    u32x4 pf;
    pipe<18, 3>([&](int j) { return lds128(kb + (j >> 1) * 4096 + 2048 + (j & 1) * 1024); },
                [&](int j, u32x4 f) {
                  const int n = j & ~1, t = j & 1;
                  if (t == 0) {
                    bg(7 + j);
                    pf[0] = pk(__builtin_amdgcn_exp2f(sc[n][0] - m), __builtin_amdgcn_exp2f(sc[n][1] - m));
                    pf[1] = pk(__builtin_amdgcn_exp2f(sc[n][2] - m), __builtin_amdgcn_exp2f(sc[n][3] - m));
                    SB();
                    bg(8 + j);
                    pf[2] = pk(__builtin_amdgcn_exp2f(sc[n + 1][0] - m), __builtin_amdgcn_exp2f(sc[n + 1][1] - m));
                    pf[3] = pk(__builtin_amdgcn_exp2f(sc[n + 1][2] - m), __builtin_amdgcn_exp2f(sc[n + 1][3] - m));
                  }
                  oa[t] = mfma(f, pf, oa[t]);
                });
    const float l = bperm(oa[1][0], 48 + i16);
    const float il = __builtin_amdgcn_rcpf(l);
    u32x4 of = {san(pk(oa[0][0] * il, oa[0][1] * il)), san(pk(oa[0][2] * il, oa[0][3] * il)),
                san(pk(oa[1][0] * il, oa[1][1] * il)), san(pk(oa[1][2] * il, oa[1][3] * il))};
    if (g4 == 3) { of[2] = 0; of[3] = 0; }
    STL128(of, RB + B_O + (u32)(mt * 16 + hc) * 1024u);
    SB();
  }
  };
  if (team) attention(std::true_type{}); else attention(std::false_type{});
  STAMP(6);
  if (team) WAITV(1);
  BAR();
  const u64 r1v = __hip_atomic_load((gu64*)(ws + RB + B_R1 + (u32)i16 * FSTR), RLX_AGENT);
#pragma unroll
  for (int n = EW2; n < 20; ++n) load_w2(n);
  SB();
  u32x4 w2f[7][4];
  auto conv_w2 = [&](int g) {
    u32x2 A[8], B[8];
#pragma unroll
    for (int j = 0; j < (g < 3 ? 8 : 4); ++j) {
      const u32x4 v = raw[8 * g + j];
#pragma unroll
      for (int d = 0; d < 2; ++d) {
        const u32 o1 = __builtin_amdgcn_update_dpp(v[d], v[2 + d], 0x128, 0xf, 0xc, false);
        const u32 o2 = __builtin_amdgcn_update_dpp(v[2 + d], v[d], 0x128, 0xf, 0x3, false);
        if (g < 3) { A[j][d] = o1; B[j][d] = o2; } else { A[j][d] = o1; A[4 + j][d] = o2; }
      }
    }
#pragma unroll
    for (int t = 0; t < 4; ++t) {
#pragma unroll
      for (int m = 0; m < 4; ++m) {
        w2f[2 * g][t][m] = __builtin_amdgcn_perm(A[2 * m + 1][t >> 1], A[2 * m][t >> 1], (t & 1) ? 0x07060302u : 0x05040100u);
        if (g < 3) w2f[2 * g + 1][t][m] = __builtin_amdgcn_perm(B[2 * m + 1][t >> 1], B[2 * m][t >> 1], (t & 1) ? 0x07060302u : 0x05040100u);
      }
      PIN(w2f[2 * g][t]);
      if (g < 3) PIN(w2f[2 * g + 1][t]);
    }
    SB();
  };
  conv_w2(0);
  if (__builtin_expect(!__all(r1v == tag), 0)) wait_flags(ws, RB + B_R1 + (u32)i16 * FSTR, tag);
  u32x2 hres[4] = {{0, 0}, {0, 0}, {0, 0}, {0, 0}};
  if (team) {
    PIN(ones);
    const u32 cvt_rd = L0 + L_W1 + (4 * g4 + q4) * 224 + 8 * p4;
    f32x4 c1[7];
#pragma unroll
    for (int t = 0; t < 7; ++t) c1[t] = f32x4{0, 0, 0, 0};
    const int lb0 = mp == 0 ? 0 : 2 * mp + 6, lb1 = 2 * mp + 7;
#pragma nounroll
    for (int k = 0; k < 4; ++k) {
      int s;
      if (k == 0) s = 1 + mp;
      else if (k == 1) {
        if (act3) await<4>(CNT + C_GO * 4);
#pragma nounroll
        for (int z = 0; z < 2; ++z) {
          const int beta = z ? lb1 : lb0;
          int ks = beta + krot; ks = ks >= 14 ? ks - 14 : ks;
#pragma unroll
          for (int pcs = 0; pcs < 7; ++pcs) {
            const int ch = pcs * 64 + lane, row = ch / 14, cc = ch - row * 14;
            glds16(p.w1 + (size_t)(ks * 32 * 3584), (u32)row * 3584u + hc * 224 + cc * 16, L0 + L_W1 + beta * 7168 + pcs * 1024);
          }
        }
        if (mp >= 2) continue;
        s = 5 + mp;
      }
      else if (k == 2) { WAITV(7); s = lb0; }
      else { WAITV(0); s = lb1; }
      u32x4 fr[7];
      const u32 a = cvt_rd + s * 7168;
#pragma unroll
      for (int t = 0; t < 7; ++t) {
        const u32 at = a + (t < 6 ? t * 32 + (hc & 1) * 32 : ((hc & 1) ? 0 : 192));
        fr[t] = trfrag(at, at + 3584);
      }
      asm volatile("s_waitcnt lgkmcnt(0)" ::: "memory");
      SB();
#pragma unroll
      for (int t = 0; t < 7; ++t) {
        sts128(L0 + L_W1 + s * 7168 + t * 1024 + lane * 16, fr[t]);
        c1[t] = mfma(fr[t], ones, c1[t]);
      }
      SB();
    }
    if (i16 == 0) {
#pragma unroll
      for (int t = 0; t < 7; ++t) stsf4(L0 + L_CSP + (mp * 7 + t) * 64 + g4 * 16, c1[t]);
    }
  } else if (act3) {
    const u32 ob = RB + B_O + (u32)((4 * q1 + w) * 16) * 1024u;
    __builtin_amdgcn_s_setprio(1);
    u32 offO[4], offL[4];
#pragma unroll
    for (int t = 0; t < 4; ++t) {
      offO[t] = L0 + L_WO + (4 * g4 + q4) * 128 + ((t ^ ((2 * g4 + (q4 >> 1)) & 3)) * 32) + 8 * p4;
      offL[t] = offO[t] - (g4 == 3 ? 2048 : 0);
      PIN(offO[t]); PIN(offL[t]);
    }
    canary(rs, ob + (u32)(lane >> 2) * 1024u + 960 + (lane & 3) * 16);
    STAMP(7);
    f32x4 acc[4];
#pragma nounroll
    for (int it = 0; it < SPIN_CAP; ++it) {
      asm volatile("" ::: "memory");
      constexpr int PD = 10;
      u32x4 ofr[16];
#pragma unroll
      for (int k = 0; k < PD; ++k) ofr[k] = LDL128(ob + k * 1024);
      SB();
      if (it == 0) sig_nw(CNT + C_GO * 4);
      u32 bad = 0;
#pragma unroll
      for (int t = 0; t < 4; ++t) acc[t] = f32x4{0, 0, 0, 0};
      pipe<64, 5>(
          [&](int j) {
            const int k = j >> 2, t = j & 3;
            const u32 a = offO[t ^ (2 * (k & 1))] + k * 3584;
            return trfrag(a, (k == 15 ? offL[t ^ 2] + k * 3584 : a) + 2048);
          },
          [&](int j, u32x4 f) {
            acc[j & 3] = mfma(f, ofr[j >> 2], acc[j & 3]);
            if ((j & 3) == 0 && (j >> 2) + PD < 16) ofr[(j >> 2) + PD] = LDL128(ob + ((j >> 2) + PD) * 1024);
            if ((j & 3) == 1) bad = chk(bad, ofr[j >> 2]);
          });
      if (__all(bad != SENT)) break;
    }
#pragma unroll
    for (int t = 0; t < 4; ++t) {
      const u32 a01 = pk(acc[t][0], acc[t][1]), a23 = pk(acc[t][2], acc[t][3]);
      const u32 h01 = pk(bflo(xr[t].x) + bflo(a01), bfhi(xr[t].x) + bfhi(a01));
      const u32 h23 = pk(bflo(xr[t].y) + bflo(a23), bfhi(xr[t].y) + bfhi(a23));
      hres[t] = u32x2{san(h01), san(h23)};
    }
#pragma unroll
    for (int u = 0; u < 2; ++u)
      STL128((u32x4{hres[2 * u].x, hres[2 * u].y, hres[2 * u + 1].x, hres[2 * u + 1].y}),
             RB + B_HH + (u32)((4 * q1 + w) * 14 + 2 * nt + u) * 1024u);
    SB();
    float s1 = 0.f, s2 = 0.f;
#pragma unroll
    for (int t = 0; t < 4; ++t) {
      const float a = bflo(hres[t].x), bb = bfhi(hres[t].x), c = bflo(hres[t].y), d = bfhi(hres[t].y);
      s1 += (a + bb) + (c + d);
      s2 += (a * a + bb * bb) + (c * c + d * d);
    }
    s1 = rows_sum(s1); s2 = rows_sum(s2);
    if (g4 == 0)
      ST64((u32x2{san(__float_as_uint(s1)), san(__float_as_uint(s2))}), RB + B_ST + (u32)((4 * q1 + w) * 8 + nt) * 128u + i16 * 8);
    SB();
    __builtin_amdgcn_s_setprio(0);
    STAMP(8);
  }
#pragma unroll
  for (int n = 20; n < 24; ++n) load_w2(n);
  SB();
  conv_w2(1);
  BAR();
  STAMP(9);
  {
    {
      const int pcs = lane < 28 ? lane : 27;
      const u32 a = L0 + L_CSP + pcs * 16;
      stsf4(L0 + L_CS1 + pcs * 16, (ldsf4(a) + ldsf4(a + 448)) + (ldsf4(a + 896) + ldsf4(a + 1344)));
    }
    const int cl = lane < 56 ? lane : lane - 56;
    const u32 hb = RB + B_HH + (u32)(mt * 14) * 1024u;
    canary(rs, hb + (u32)(cl >> 2) * 1024u + 960 + (cl & 3) * 16);
    STAMP(10);
    u32x4 hf[14];
    f32x4 acc[7];
    u32 w1a = L0 + L_W1 + lane * 16, w1b = w1a + 9 * 7168;
    PIN(w1a); PIN(w1b);
    const u32 sto0 = RB + B_ST + (u32)(mt * 8 + g4) * 128u + i16 * 8, sto1 = RB + B_ST + (u32)(mt * 8 + (g4 < 3 ? g4 + 4 : 0)) * 128u + i16 * 8;
    const u32 hlo = hb + krot * 1024, hhi = hb - krot * 1024;
    auto w1rd = [&](int s, int t) { return lds128((s < 9 ? w1a + s * 7168 : w1b + (s - 9) * 7168) + t * 1024); };
    u32x2 sa, sb;
#pragma nounroll
    for (int it = 0; it < SPIN_CAP; ++it) {
      asm volatile("" ::: "memory");
      constexpr int PD = 6;
#pragma unroll
      for (int s = 0; s < PD; ++s) hf[s] = LDL128((s < 7 ? hlo : hhi) + s * 1024);
      SB();
      u32 bad = 0;
#pragma unroll
      for (int t = 0; t < 4; ++t) acc[t] = f32x4{0, 0, 0, 0};
      pipe<56, 4>([&](int j) { return w1rd(j >> 2, j & 3); },
                  [&](int j, u32x4 f) {
                    acc[j & 3] = mfma(f, hf[j >> 2], acc[j & 3]);
                    if ((j & 3) == 0 && (j >> 2) + PD < 14) hf[(j >> 2) + PD] = LDL128(((j >> 2) + PD < 7 ? hlo : hhi) + ((j >> 2) + PD) * 1024);
                    if (j == 36) sa = LD64(sto0);
                    if (j == 40) sb = LD64(sto1);
                    if ((j & 3) == 1) bad = chk(bad, hf[j >> 2]);
                  });
      bad = umax3(umax3(bad, sa.x, sa.y), sb.x, sb.y);
      if (__all(bad != SENT)) break;
    }
    const float s1 = rows_sum(__uint_as_float(sa.x) + (g4 < 3 ? __uint_as_float(sb.x) : 0.f));
    const float s2 = rows_sum(__uint_as_float(sa.y) + (g4 < 3 ? __uint_as_float(sb.y) : 0.f));
    const float mean = s1 * (1.0f / D);
    const float rstd = __builtin_amdgcn_rsqf(fmaxf(s2 * (1.0f / D) - mean * mean, 0.f) + 1e-5f);
    const float nmr = -mean * rstd;
    const u32 fb = RB + B_F + (u32)(mt * 8 + (hc >> 1)) * 7168u;
    auto epi = [&](int t) {
      const f32x4 c = ldsf4(L0 + L_CS1 + t * 64 + g4 * 16);
      return u32x2{relu2(pk(rstd * acc[t][0] + nmr * c[0], rstd * acc[t][1] + nmr * c[1])),
                   relu2(pk(rstd * acc[t][2] + nmr * c[2], rstd * acc[t][3] + nmr * c[3]))};
    };
#pragma unroll
    for (int t = 4; t < 7; ++t) acc[t] = f32x4{0, 0, 0, 0};
    pipe<42, 4>([&](int j) { const int s = j / 3, t = j - 3 * s; return w1rd(s, 4 + t); },
                [&](int j, u32x4 f) {
                  const int s = j / 3, t = j - 3 * s;
                  acc[4 + t] = mfma(f, hf[s], acc[4 + t]);
                  if (j == 2 || j == 10) {
                    const int k = j == 2 ? 0 : 1;
                    const u32x2 a = epi(2 * k), c = epi(2 * k + 1);
                    STL128((u32x4{a.x, a.y, c.x, c.y}), fb + (hc & 1) * 4096 + k * 1024);
                  }
                  if (j >= 12 && j < 24 && t == 0) load_w2(24 + (s - 4));
                });
    {
      const u32x2 a = epi(4), c = epi(5), d = epi(6);
      STL128((u32x4{a.x, a.y, c.x, c.y}), fb + (hc & 1) * 4096 + 2048);
      STL64(d, fb + 3072 + (hc & 1) * 512);
    }
    SB();
    STAMP(11);
  }
  conv_w2(2);
  WAITV(0);
  set_flag(ws, RB + B_F4 + (u32)(mt * 16 + hc) * FSTR, tag, lane);
  if (act3) {
    conv_w2(3);
    BAR();
    if (w < 4) {
#pragma unroll
      for (int t = 0; t < 4; ++t) sts64(L0 + L_HRES + w * 2048 + i16 * 128 + t * 32 + g4 * 8, hres[t]);
    }
    wait_flags(ws, RB + B_F4 + (u32)((4 * q1 + ((lane >> 1) & 3)) * 16 + 2 * kr + (lane & 1)) * FSTR, tag);
    STAMP(12);
    {
      const u32 pw = L0 + L_PART + w * 16384 + lane * 8;
      constexpr int PD = 10, RG = PD + 1;
      u32x4 ff[RG];
      auto ldf = [&](int j) {
        const int m = j / 7, k = j - 7 * m;
        const u32 fb = RB + B_F + (u32)((4 * q1 + m) * 8 + kr) * 7168u;
        if (k == 3) {
          const u32x2 a = LDL64(fb + 3072), c = LDL64(fb + 3584);
          ff[j % RG] = u32x4{a.x, a.y, c.x, c.y};
        } else ff[j % RG] = LDL128(fb + k * 1024);
      };
#pragma unroll
      for (int j = 0; j < PD; ++j) ldf(j);
      SB();
      f32x4 acc[2][4];
      auto wr = [&](int m, int t) {
        stsf2(pw + (m * 4 + t) * 1024, f32x2{acc[m & 1][t][0], acc[m & 1][t][1]});
        stsf2(pw + (m * 4 + t) * 1024 + 512, f32x2{acc[m & 1][t][2], acc[m & 1][t][3]});
      };
#pragma unroll
      for (int j = 0; j < 28; ++j) {
        const int m = j / 7, k = j - 7 * m;
        if (j + PD < 28) ldf(j + PD);
#pragma unroll
        for (int t = 0; t < 4; ++t) acc[m & 1][t] = mfma(w2f[k][t], ff[j % RG], k == 0 ? f32x4{0, 0, 0, 0} : acc[m & 1][t]);
        if (m > 0 && k >= 1 && k <= 4) wr(m - 1, k - 1);
        SB();
      }
#pragma unroll
      for (int t = 0; t < 4; ++t) wr(3, t);
    }
    STAMP(13);
    BAR();
    {
      const int m = w >> 1, rh = w & 1;
      float o[2][4];
#pragma unroll
      for (int t = 0; t < 4; ++t) {
        f32x2 s = {0, 0};
#pragma unroll
        for (int ww = 0; ww < 8; ++ww) s += ldsf2(L0 + L_PART + ((ww * 4 + m) * 4 + t) * 1024 + rh * 512 + lane * 8);
        o[0][t] = s.x; o[1][t] = s.y;
      }
      u32 y0[2] = {pk(o[0][0], o[0][1]), pk(o[0][2], o[0][3])}, y1[2] = {pk(o[1][0], o[1][1]), pk(o[1][2], o[1][3])};
      u32x4 y;
#pragma unroll
      for (int d = 0; d < 2; ++d) {
        auto sw = __builtin_amdgcn_permlane32_swap(y0[d], y1[d], false, false);
        y[d] = sw[0]; y[2 + d] = sw[1];
      }
      const int colb = (32 * (g4 & 1) + 8 * (2 * rh + (g4 >> 1))) * 2;
      const u32x4 hr = lds128(L0 + L_HRES + m * 2048 + i16 * 128 + colb);
      u32x4 res;
#pragma unroll
      for (int e = 0; e < 4; ++e) res[e] = pk(bflo(hr[e]) + bflo(y[e]), bfhi(hr[e]) + bfhi(y[e]));
      *(GAS u32x4*)(p.out + (size_t)(16 * (8 * rb + 4 * q1 + m) + i16) * 896 + nt * 128 + colb) = res;
    }
  } else if (b == 255 && w == 0) {
#pragma nounroll
    for (int k = 0; k < 4; ++k) wait_flags(ws, (u32)(4 * k + g4) * RBS + B_R1 + (u32)i16 * FSTR, tag);
    if (lane == 0) __hip_atomic_store((gu64*)(ws + WS_CTL), tag, RLX_AGENT);
  }
  asm volatile(".Lfb_end_a:" ::: "memory");
}
}

extern "C" void kernel_launch(void* const* d_in, const int* in_sizes, int n_in, void* d_out, int out_size, void* d_ws,
                              size_t ws_size, hipStream_t stream) {
  fb::Params p;
  p.x = (const char*)d_in[0]; p.wqkv = (const char*)d_in[1]; p.wo = (const char*)d_in[2];
  p.w1 = (const char*)d_in[4]; p.w2 = (const char*)d_in[5];
  p.out = (char*)d_out; p.ws = (char*)d_ws;
  fb::fused_block<<<256, 512, 0, stream>>>(p);
}
```
